```python
import torch

def _softmax(x, axis=-1):
    return torch.softmax(x, dim=axis)
def _gelu(x):
    return torch.nn.functional.gelu(x, approximate="tanh")
def _silu(x):
    return torch.nn.functional.silu(x)
def _sigmoid(x):
    return torch.sigmoid(x)
def _layernorm(x, axis=-1, eps=1e-5):
    m = x.mean(dim=axis, keepdim=True)
    v = x.var(dim=axis, keepdim=True, unbiased=False)
    return (x - m) / torch.sqrt(v + eps)
def _scatter_add(tgt, idx, src, axis=0):
    return tgt.clone().index_add_(axis, idx, src)
def _segment_sum(src, seg, n):
    out = torch.zeros((n,) + src.shape[1:], dtype=src.dtype)
    return out.index_add_(0, seg, src)
def _pad(x, pw):
    flat = tuple(v for pair in reversed(pw) for v in pair)
    return torch.nn.functional.pad(x, flat)

kernel_name = 'recurrent_scan__linear_attn_block__dense_activation_gelu__prompt'

def setup_inputs():

    _w = (lambda a: (lambda t: t.to(torch.bfloat16) if t.is_floating_point() else t)(torch.as_tensor(a)))
    def _A_setup():
        B, S, D = 1, 2048, 576
        return {
            "x": _w((lambda *s: torch.randn(*s, dtype=torch.bfloat16) * 0.05)(B, S, D)),
            "wqkv": _w((lambda *s: torch.randn(*s, dtype=torch.bfloat16) * 0.2)(D, 3 * D)),
            "wo": _w((lambda *s: torch.randn(*s, dtype=torch.bfloat16) * 0.2)(D, D)),
        }
    _A_in = _A_setup()

    def _B_setup():
        B, S, D, F = 1, 2048, 576, 4608
        return {
            "x": _w((lambda *s: torch.randn(*s, dtype=torch.bfloat16) * 0.05)(B, S, D)),
            "w1": _w((lambda *s: torch.randn(*s, dtype=torch.bfloat16) * 0.4)(D, F) * (D ** -0.5)),
            "b1": _w((lambda *s: torch.randn(*s, dtype=torch.bfloat16) * 12.8)(F) * 0.01),
            "w2": _w((lambda *s: torch.randn(*s, dtype=torch.bfloat16) * 0.2)(F, D) * (F ** -0.5)),
            "b2": _w((lambda *s: torch.randn(*s, dtype=torch.bfloat16) * 1.6)(D) * 0.01),
        }
    _B_in = _B_setup()

    return {
        "A_x": _A_in["x"],
        "A_wqkv": _A_in["wqkv"],
        "A_wo": _A_in["wo"],
        "B_x": _B_in["x"],
        "B_w1": _B_in["w1"],
        "B_b1": _B_in["b1"],
        "B_w2": _B_in["w2"],
        "B_b2": _B_in["b2"],
    }

def reference(A_x, A_wqkv, A_wo, B_x, B_w1, B_b1, B_w2, B_b2):
    def _A_ref(x, wqkv, wo):
        B, S, D = x.shape
        H = 32; Dh = D // H
        qkv = (lambda x, *d: x.permute(*d))((x @ wqkv).reshape(B, S, 3, H, Dh), 2, 0, 3, 1, 4)
        q, k, v = qkv[0], qkv[1], qkv[2]
        phi = lambda t: torch.where(t > 0, t + 1, torch.exp(t)) + 1e-6
        pq, pk = phi(q), phi(k)
        kv = torch.einsum("bhsd,bhse->bhde", pk, v)
        z = torch.einsum("bhsd->bhd", pk)
        num = torch.einsum("bhqd,bhde->bhqe", pq, kv)
        den = torch.einsum("bhqd,bhd->bhq", pq, z)[..., None] + 1e-6
        o = num / den
        return (lambda x, *d: x.permute(*d))(o, 0, 2, 1, 3).reshape(B, S, D) @ wo

    def _B_ref(x, w1, b1, w2, b2):
        h = torch.einsum("bsd,df->bsf", x, w1) + b1
        h = _gelu(h)
        return torch.einsum("bsf,fd->bsd", h, w2) + b2

    x = A_x
    xn = _layernorm(x, axis=-1)
    s = _A_ref(xn, A_wqkv, A_wo)
    g = _silu(_B_ref(xn, B_w1, B_b1, B_w2, B_b2))
    return x + s * g
```

```cpp
#include <hip/hip_runtime.h>
#include <hip/hip_bf16.h>
typedef unsigned short u16;
__device__ __forceinline__ float bf2f(u16 b){ return __uint_as_float(((unsigned)b)<<16); }
__device__ __forceinline__ u16 f2bf(float f){ unsigned u=__float_as_uint(f); u += 0x7fffu + ((u>>16)&1u); return (u16)(u>>16); }
constexpr int S=2048,D=576,F=4608,H=32,DH=18;

__global__ void ln_k(const u16* x, u16* xn){
  int r=blockIdx.x; int t=threadIdx.x;
  float v[9]; float s=0;
  for(int i=0;i<9;i++){ v[i]=bf2f(x[r*D+t+64*i]); s+=v[i]; }
  for(int o=32;o;o>>=1) s+=__shfl_xor(s,o);
  float m=s/D; float q=0;
  for(int i=0;i<9;i++){ float d=v[i]-m; q+=d*d; }
  for(int o=32;o;o>>=1) q+=__shfl_xor(q,o);
  float rs=rsqrtf(q/D+1e-5f);
  for(int i=0;i<9;i++) xn[r*D+t+64*i]=f2bf((v[i]-m)*rs);
}
template<int MODE> __global__ void gemm_k(const u16* A,const u16* B,int M,int N,int K,const u16* bias,u16* C,float* Cf){
  __shared__ float As[16][65]; __shared__ float Bs[16][65];
  int tx=threadIdx.x&15, ty=threadIdx.x>>4; int m0=blockIdx.y*64,n0=blockIdx.x*64;
  float acc[4][4]={};
  for(int k0=0;k0<K;k0+=16){
    for(int i=threadIdx.x;i<1024;i+=256){ int r=i>>4,c=i&15; As[c][r]=bf2f(A[(size_t)(m0+r)*K+k0+c]); int kk=i>>6,nn=i&63; Bs[kk][nn]=bf2f(B[(size_t)(k0+kk)*N+n0+nn]); }
    __syncthreads();
    for(int k=0;k<16;k++){ float a[4],b[4]; for(int i=0;i<4;i++){a[i]=As[k][ty*4+i]; b[i]=Bs[k][tx*4+i];}
      for(int i=0;i<4;i++)for(int j=0;j<4;j++) acc[i][j]+=a[i]*b[j]; }
    __syncthreads();
  }
  for(int i=0;i<4;i++)for(int j=0;j<4;j++){ int m=m0+ty*4+i,n=n0+tx*4+j; float v=acc[i][j];
    if(MODE==1){ v+=bf2f(bias[n]); float u=0.7978845608f*(v+0.044715f*v*v*v); v=0.5f*v*(1.f+tanhf(u)); }
    if(MODE==2){ v+=bf2f(bias[n]); }
    if(MODE==2||MODE==3) Cf[(size_t)m*N+n]=v; else C[(size_t)m*N+n]=f2bf(v);
  }
}
__device__ __forceinline__ float phi(float t){ return (t>0? t+1.f: __expf(t))+1e-6f; }
__global__ void kv_k(const u16* qkv,float* kv,float* z){
  int h=blockIdx.x; int t=threadIdx.x;
  float acc=0;
  if(t<324){ int d=t/18,e=t%18; for(int s=0;s<S;s++) acc+=bf2f(f2bf(phi(bf2f(qkv[s*1728+576+h*18+d]))))*bf2f(qkv[s*1728+1152+h*18+e]); kv[h*324+t]=acc; }
  else if(t<342){ int d=t-324; for(int s=0;s<S;s++) acc+=bf2f(f2bf(phi(bf2f(qkv[s*1728+576+h*18+d])))); z[h*18+d]=acc; }
}
__global__ void o_k(const u16* qkv,const float* kv,const float* z,u16* o){
  int s=blockIdx.x; int c=threadIdx.x; if(c>=D) return; int h=c/18,e=c%18;
  float num=0,den=0; for(int d=0;d<18;d++){ float p=bf2f(f2bf(phi(bf2f(qkv[s*1728+h*18+d])))); num+=p*kv[h*324+d*18+e]; den+=p*z[h*18+d]; }
  o[s*D+c]=f2bf(num/(den+1e-6f));
}
__global__ void fin_k(const u16* x,const float* s,const float* g,u16* out){
  int i=blockIdx.x*256+threadIdx.x; float gg=g[i]; float sl=gg/(1.f+__expf(-gg)); out[i]=f2bf(bf2f(x[i])+s[i]*sl);
}
extern "C" void kernel_launch(void* const* d_in, const int* in_sizes, int n_in, void* d_out, int out_size, void* d_ws, size_t ws_size, hipStream_t stream){
  const u16* x=(const u16*)d_in[0]; const u16* wqkv=(const u16*)d_in[1]; const u16* wo=(const u16*)d_in[2];
  const u16* w1=(const u16*)d_in[4]; const u16* b1=(const u16*)d_in[5]; const u16* w2=(const u16*)d_in[6]; const u16* b2=(const u16*)d_in[7];
  char* ws=(char*)d_ws; u16* xn=(u16*)ws; u16* qkv=(u16*)(ws+(4<<20)); u16* h=(u16*)(ws+(16<<20)); u16* o=(u16*)(ws+(40<<20));
  float* kv=(float*)(ws+(44<<20)); float* z=(float*)(ws+(45<<20)); float* sf=(float*)(ws+(48<<20)); float* gf=(float*)(ws+(56<<20));
  ln_k<<<S,64,0,stream>>>(x,xn);
  gemm_k<0><<<dim3(1728/64,S/64),256,0,stream>>>(xn,wqkv,S,1728,D,nullptr,qkv,nullptr);
  gemm_k<1><<<dim3(F/64,S/64),256,0,stream>>>(xn,w1,S,F,D,b1,h,nullptr);
  kv_k<<<H,384,0,stream>>>(qkv,kv,z);
  o_k<<<S,576,0,stream>>>(qkv,kv,z,o);
  gemm_k<3><<<dim3(D/64,S/64),256,0,stream>>>(o,wo,S,D,D,nullptr,nullptr,sf);
  gemm_k<2><<<dim3(D/64,S/64),256,0,stream>>>(h,w2,S,D,F,b2,nullptr,gf);
  fin_k<<<S*D/256,256,0,stream>>>(x,sf,gf,(u16*)d_out);
}
```

```cpp
#include <hip/hip_runtime.h>
#include <hip/hip_bf16.h>
typedef unsigned short u16;
__device__ __forceinline__ float bf2f(u16 b){ return __uint_as_float(((unsigned)b)<<16); }
__device__ __forceinline__ u16 f2bf(float f){ unsigned u=__float_as_uint(f); u += 0x7fffu + ((u>>16)&1u); return (u16)(u>>16); }
constexpr int S=2048,D=576,F=4608,H=32,DH=18;

__global__ void ln_k(const u16* x, u16* xn){
  int r=blockIdx.x; int t=threadIdx.x;
  float v[9]; float s=0;
  for(int i=0;i<9;i++){ v[i]=bf2f(x[r*D+t+64*i]); s+=v[i]; }
  for(int o=32;o;o>>=1) s+=__shfl_xor(s,o);
  float m=s/D; float q=0;
  for(int i=0;i<9;i++){ float d=v[i]-m; q+=d*d; }
  for(int o=32;o;o>>=1) q+=__shfl_xor(q,o);
  float rs=rsqrtf(q/D+1e-5f);
  for(int i=0;i<9;i++) xn[r*D+t+64*i]=f2bf((v[i]-m)*rs);
}
template<int MODE> __global__ void gemm_k(const u16* A,const u16* B,int M,int N,int K,const u16* bias,u16* C,float* Cf){
  __shared__ float As[16][65]; __shared__ float Bs[16][65];
  int tx=threadIdx.x&15, ty=threadIdx.x>>4; int m0=blockIdx.y*64,n0=blockIdx.x*64;
  float acc[4][4]={};
  for(int k0=0;k0<K;k0+=16){
    for(int i=threadIdx.x;i<1024;i+=256){ int r=i>>4,c=i&15; As[c][r]=bf2f(A[(size_t)(m0+r)*K+k0+c]); int kk=i>>6,nn=i&63; Bs[kk][nn]=bf2f(B[(size_t)(k0+kk)*N+n0+nn]); }
    __syncthreads();
    for(int k=0;k<16;k++){ float a[4],b[4]; for(int i=0;i<4;i++){a[i]=As[k][ty*4+i]; b[i]=Bs[k][tx*4+i];}
      for(int i=0;i<4;i++)for(int j=0;j<4;j++) acc[i][j]+=a[i]*b[j]; }
    __syncthreads();
  }
  for(int i=0;i<4;i++)for(int j=0;j<4;j++){ int m=m0+ty*4+i,n=n0+tx*4+j; float v=acc[i][j];
    if(MODE==1){ v+=bf2f(bias[n]); float u=0.7978845608f*(v+0.044715f*v*v*v); v=0.5f*v*(1.f+tanhf(u)); }
    if(MODE==2){ v+=bf2f(bias[n]); }
    if(MODE==2||MODE==3) Cf[(size_t)m*N+n]=v; else C[(size_t)m*N+n]=f2bf(v);
  }
}
__device__ __forceinline__ float phi(float t){ return (t>0? t+1.f: __expf(t))+1e-6f; }
__global__ void kv_k(const u16* qkv,float* kv,float* z){
  int h=blockIdx.x; int t=threadIdx.x;
  float acc=0;
  if(t<324){ int d=t/18,e=t%18; for(int s=0;s<S;s++) acc+=bf2f(f2bf(phi(bf2f(qkv[s*1728+576+h*18+d]))))*bf2f(qkv[s*1728+1152+h*18+e]); kv[h*324+t]=acc; }
  else if(t<342){ int d=t-324; for(int s=0;s<S;s++) acc+=bf2f(f2bf(phi(bf2f(qkv[s*1728+576+h*18+d])))); z[h*18+d]=acc; }
}
__global__ void o_k(const u16* qkv,const float* kv,const float* z,u16* o){
  int s=blockIdx.x; int c=threadIdx.x; if(c>=D) return; int h=c/18,e=c%18;
  float num=0,den=0; for(int d=0;d<18;d++){ float p=bf2f(f2bf(phi(bf2f(qkv[s*1728+h*18+d])))); num+=p*kv[h*324+d*18+e]; den+=p*z[h*18+d]; }
  o[s*D+c]=f2bf(num/(den+1e-6f));
}
__global__ void fin_k(const u16* x,const float* s,const float* g,u16* out){
  int i=blockIdx.x*256+threadIdx.x; float gg=g[i]; float sl=gg/(1.f+__expf(-gg)); out[i]=f2bf(bf2f(x[i])+s[i]*sl);
}
typedef unsigned v4u __attribute__((ext_vector_type(4)));
typedef unsigned long long u64;
__global__ __launch_bounds__(512) void probe_rd(const char* p0,int n0,const char* p1,int n1,const char* p2,int n2,u64* ts,unsigned* sink,int shared_all){
  u64 t0=__builtin_amdgcn_s_memrealtime();
  unsigned acc=0;
  int nb = shared_all? 32: 256; int b = shared_all? (blockIdx.x>>3) : blockIdx.x;
  const char* ps[3]={p0,p1,p2}; int ns[3]={n0,n1,n2};
  #pragma unroll
  for(int a=0;a<3;a++){
    int n16=ns[a]/16; int per=n16/nb; const v4u* q=(const v4u*)ps[a]+(size_t)b*per;
    v4u v[8];
    for(int i=threadIdx.x;i<per;i+=512*8){
      #pragma unroll
      for(int j=0;j<8;j++){ int k=i+j*512; v[j]= k<per? q[k]: v4u{0,0,0,0}; }
      #pragma unroll
      for(int j=0;j<8;j++) acc^=v[j][0];
    }
  }
  if(acc==0x12345678) sink[0]=acc;
  __syncthreads();
  if(threadIdx.x==0){ ts[2*blockIdx.x]=t0; ts[2*blockIdx.x+1]=__builtin_amdgcn_s_memrealtime(); }
}
template<int AUX> __global__ __launch_bounds__(512) void probe_wr(char* p,int nkb,u64* ts){
  u64 t0=__builtin_amdgcn_s_memrealtime();
  int wave=__builtin_amdgcn_readfirstlane(threadIdx.x>>6), lane=threadIdx.x&63;
  __amdgpu_buffer_rsrc_t r=__builtin_amdgcn_make_buffer_rsrc((void*)(p+(size_t)blockIdx.x*nkb*1024),0,0x7fffffff,0x00020000);
  int n=nkb/8; v4u v={(unsigned)lane,(unsigned)t0,3u,4u};
  for(int i=0;i<n;i++) __builtin_amdgcn_raw_buffer_store_b128(v,r,lane*16,(wave*n+i)*1024,AUX);
  asm volatile("s_waitcnt vmcnt(0)":::"memory");
  __syncthreads();
  if(threadIdx.x==0){ ts[2*blockIdx.x]=t0; ts[2*blockIdx.x+1]=__builtin_amdgcn_s_memrealtime(); }
}
__global__ void probe_spin(const u64* ts,int mode,int mult){
  u64 now=__builtin_amdgcn_s_memrealtime();
  u64 mn=~0ull,mx=0;
  for(int i=threadIdx.x;i<256;i+=64){ u64 a=ts[2*i],b=ts[2*i+1]; mn=a<mn?a:mn; mx=b>mx?b:mx; }
  for(int o=32;o;o>>=1){ u64 a=__shfl_xor(mn,o),b=__shfl_xor(mx,o); mn=a<mn?a:mn; mx=b>mx?b:mx; }
  u64 now0=__shfl(now,0);
  u64 d = mode==0? mx-mn : (mode==1? now0-mn : now0-mx); if(d>5000) d=5000;
  u64 t=__builtin_amdgcn_s_memrealtime(); while(__builtin_amdgcn_s_memrealtime()-t<d*mult) __builtin_amdgcn_s_sleep(8);
}
extern "C" void kernel_launch(void* const* d_in, const int* in_sizes, int n_in, void* d_out, int out_size, void* d_ws, size_t ws_size, hipStream_t stream){
  const u16* x=(const u16*)d_in[0]; const u16* wqkv=(const u16*)d_in[1]; const u16* wo=(const u16*)d_in[2];
  const u16* w1=(const u16*)d_in[4]; const u16* b1=(const u16*)d_in[5]; const u16* w2=(const u16*)d_in[6]; const u16* b2=(const u16*)d_in[7];
  char* ws=(char*)d_ws; u16* xn=(u16*)ws; u16* qkv=(u16*)(ws+(4<<20)); u16* h=(u16*)(ws+(16<<20)); u16* o=(u16*)(ws+(40<<20));
  float* kv=(float*)(ws+(44<<20)); float* z=(float*)(ws+(45<<20)); float* sf=(float*)(ws+(48<<20)); float* gf=(float*)(ws+(56<<20));
  u64* ts=(u64*)(ws+(100<<20)); unsigned* sink=(unsigned*)(ws+(101<<20));
  probe_rd<<<256,512,0,stream>>>((const char*)x,0,(const char*)wqkv,0,(const char*)w1,0,ts,sink,0); probe_spin<<<1,64,0,stream>>>(ts,0,200);
  ln_k<<<S,64,0,stream>>>(x,xn);
  gemm_k<0><<<dim3(1728/64,S/64),256,0,stream>>>(xn,wqkv,S,1728,D,nullptr,qkv,nullptr);
  gemm_k<1><<<dim3(F/64,S/64),256,0,stream>>>(xn,w1,S,F,D,b1,h,nullptr);
  kv_k<<<H,384,0,stream>>>(qkv,kv,z);
  o_k<<<S,576,0,stream>>>(qkv,kv,z,o);
  gemm_k<3><<<dim3(D/64,S/64),256,0,stream>>>(o,wo,S,D,D,nullptr,nullptr,sf);
  gemm_k<2><<<dim3(D/64,S/64),256,0,stream>>>(h,w2,S,D,F,b2,nullptr,gf);
  fin_k<<<S*D/256,256,0,stream>>>(x,sf,gf,(u16*)d_out);
}
```

```cpp
#include <hip/hip_runtime.h>
#include <hip/hip_bf16.h>
typedef unsigned short u16;
__device__ __forceinline__ float bf2f(u16 b){ return __uint_as_float(((unsigned)b)<<16); }
__device__ __forceinline__ u16 f2bf(float f){ unsigned u=__float_as_uint(f); u += 0x7fffu + ((u>>16)&1u); return (u16)(u>>16); }
constexpr int S=2048,D=576,F=4608,H=32,DH=18;

__global__ void ln_k(const u16* x, u16* xn){
  int r=blockIdx.x; int t=threadIdx.x;
  float v[9]; float s=0;
  for(int i=0;i<9;i++){ v[i]=bf2f(x[r*D+t+64*i]); s+=v[i]; }
  for(int o=32;o;o>>=1) s+=__shfl_xor(s,o);
  float m=s/D; float q=0;
  for(int i=0;i<9;i++){ float d=v[i]-m; q+=d*d; }
  for(int o=32;o;o>>=1) q+=__shfl_xor(q,o);
  float rs=rsqrtf(q/D+1e-5f);
  for(int i=0;i<9;i++) xn[r*D+t+64*i]=f2bf((v[i]-m)*rs);
}
template<int MODE> __global__ void gemm_k(const u16* A,const u16* B,int M,int N,int K,const u16* bias,u16* C,float* Cf){
  __shared__ float As[16][65]; __shared__ float Bs[16][65];
  int tx=threadIdx.x&15, ty=threadIdx.x>>4; int m0=blockIdx.y*64,n0=blockIdx.x*64;
  float acc[4][4]={};
  for(int k0=0;k0<K;k0+=16){
    for(int i=threadIdx.x;i<1024;i+=256){ int r=i>>4,c=i&15; As[c][r]=bf2f(A[(size_t)(m0+r)*K+k0+c]); int kk=i>>6,nn=i&63; Bs[kk][nn]=bf2f(B[(size_t)(k0+kk)*N+n0+nn]); }
    __syncthreads();
    for(int k=0;k<16;k++){ float a[4],b[4]; for(int i=0;i<4;i++){a[i]=As[k][ty*4+i]; b[i]=Bs[k][tx*4+i];}
      for(int i=0;i<4;i++)for(int j=0;j<4;j++) acc[i][j]+=a[i]*b[j]; }
    __syncthreads();
  }
  for(int i=0;i<4;i++)for(int j=0;j<4;j++){ int m=m0+ty*4+i,n=n0+tx*4+j; float v=acc[i][j];
    if(MODE==1){ v+=bf2f(bias[n]); float u=0.7978845608f*(v+0.044715f*v*v*v); v=0.5f*v*(1.f+tanhf(u)); }
    if(MODE==2){ v+=bf2f(bias[n]); }
    if(MODE==2||MODE==3) Cf[(size_t)m*N+n]=v; else C[(size_t)m*N+n]=f2bf(v);
  }
}
__device__ __forceinline__ float phi(float t){ return (t>0? t+1.f: __expf(t))+1e-6f; }
__global__ void kv_k(const u16* qkv,float* kv,float* z){
  int h=blockIdx.x; int t=threadIdx.x;
  float acc=0;
  if(t<324){ int d=t/18,e=t%18; for(int s=0;s<S;s++) acc+=bf2f(f2bf(phi(bf2f(qkv[s*1728+576+h*18+d]))))*bf2f(qkv[s*1728+1152+h*18+e]); kv[h*324+t]=acc; }
  else if(t<342){ int d=t-324; for(int s=0;s<S;s++) acc+=bf2f(f2bf(phi(bf2f(qkv[s*1728+576+h*18+d])))); z[h*18+d]=acc; }
}
__global__ void o_k(const u16* qkv,const float* kv,const float* z,u16* o){
  int s=blockIdx.x; int c=threadIdx.x; if(c>=D) return; int h=c/18,e=c%18;
  float num=0,den=0; for(int d=0;d<18;d++){ float p=bf2f(f2bf(phi(bf2f(qkv[s*1728+h*18+d])))); num+=p*kv[h*324+d*18+e]; den+=p*z[h*18+d]; }
  o[s*D+c]=f2bf(num/(den+1e-6f));
}
__global__ void fin_k(const u16* x,const float* s,const float* g,u16* out){
  int i=blockIdx.x*256+threadIdx.x; float gg=g[i]; float sl=gg/(1.f+__expf(-gg)); out[i]=f2bf(bf2f(x[i])+s[i]*sl);
}
typedef unsigned v4u __attribute__((ext_vector_type(4)));
typedef unsigned long long u64;
__global__ __launch_bounds__(512) void probe_rd(const char* p0,int n0,const char* p1,int n1,const char* p2,int n2,u64* ts,unsigned* sink,int shared_all){
  u64 t0=__builtin_amdgcn_s_memrealtime();
  unsigned acc=0;
  int nb = shared_all? 32: 256; int b = shared_all? (blockIdx.x>>3) : blockIdx.x;
  const char* ps[3]={p0,p1,p2}; int ns[3]={n0,n1,n2};
  #pragma unroll
  for(int a=0;a<3;a++){
    int n16=ns[a]/16; int per=n16/nb; const v4u* q=(const v4u*)ps[a]+(size_t)b*per;
    v4u v[8];
    for(int i=threadIdx.x;i<per;i+=512*8){
      #pragma unroll
      for(int j=0;j<8;j++){ int k=i+j*512; v[j]= k<per? q[k]: v4u{0,0,0,0}; }
      #pragma unroll
      for(int j=0;j<8;j++) acc^=v[j][0];
    }
  }
  if(acc==0x12345678) sink[0]=acc;
  __syncthreads();
  if(threadIdx.x==0){ ts[2*blockIdx.x]=t0; ts[2*blockIdx.x+1]=__builtin_amdgcn_s_memrealtime(); }
}
template<int AUX> __global__ __launch_bounds__(512) void probe_wr(char* p,int nkb,u64* ts){
  u64 t0=__builtin_amdgcn_s_memrealtime();
  int wave=__builtin_amdgcn_readfirstlane(threadIdx.x>>6), lane=threadIdx.x&63;
  __amdgpu_buffer_rsrc_t r=__builtin_amdgcn_make_buffer_rsrc((void*)(p+(size_t)blockIdx.x*nkb*1024),0,0x7fffffff,0x00020000);
  int n=nkb/8; v4u v={(unsigned)lane,(unsigned)t0,3u,4u};
  for(int i=0;i<n;i++) __builtin_amdgcn_raw_buffer_store_b128(v,r,lane*16,(wave*n+i)*1024,AUX);
  asm volatile("s_waitcnt vmcnt(0)":::"memory");
  __syncthreads();
  if(threadIdx.x==0){ ts[2*blockIdx.x]=t0; ts[2*blockIdx.x+1]=__builtin_amdgcn_s_memrealtime(); }
}
__global__ void probe_spin(const u64* ts,int mode,int mult){
  u64 now=__builtin_amdgcn_s_memrealtime();
  u64 mn=~0ull,mx=0;
  for(int i=threadIdx.x;i<256;i+=64){ u64 a=ts[2*i],b=ts[2*i+1]; mn=a<mn?a:mn; mx=b>mx?b:mx; }
  for(int o=32;o;o>>=1){ u64 a=__shfl_xor(mn,o),b=__shfl_xor(mx,o); mn=a<mn?a:mn; mx=b>mx?b:mx; }
  u64 now0=__shfl(now,0);
  u64 d = mode==0? mx-mn : (mode==1? now0-mn : now0-mx); if(d>5000) d=5000;
  u64 t=__builtin_amdgcn_s_memrealtime(); while(__builtin_amdgcn_s_memrealtime()-t<d*mult) __builtin_amdgcn_s_sleep(8);
}
template<int N> struct Rep { static __device__ __forceinline__ void run(float& a,float& b){
  asm volatile("v_fma_f32 %0, %0, %1, %1\n v_fma_f32 %1, %0, %1, %0\n v_fma_f32 %0, %0, %1, %1\n v_fma_f32 %1, %0, %1, %0\n v_fma_f32 %0, %0, %1, %1\n v_fma_f32 %1, %0, %1, %0\n v_fma_f32 %0, %0, %1, %1\n v_fma_f32 %1, %0, %1, %0\n":"+v"(a),"+v"(b)); Rep<N-1>::run(a,b);} };
template<> struct Rep<0>{ static __device__ __forceinline__ void run(float&,float&){} };
template<int NB> __global__ __launch_bounds__(512) void probe_code(u64* ts,float* sink,int loops){
  u64 t0=__builtin_amdgcn_s_memrealtime();
  float a=threadIdx.x*1e-9f,b=0.5f;
  for(int l=0;l<loops;l++){ Rep<NB/4>::run(a,b); Rep<NB/4>::run(a,b); Rep<NB/4>::run(a,b); Rep<NB/4>::run(a,b); }
  if(a==123.f) sink[0]=a+b;
  __syncthreads();
  if(threadIdx.x==0){ ts[2*blockIdx.x]=t0; ts[2*blockIdx.x+1]=__builtin_amdgcn_s_memrealtime(); }
}
__global__ void probe_spin2(const u64* ts,int mult){
  u64 mx=0;
  for(int i=threadIdx.x;i<256;i+=64){ u64 d=ts[2*i+1]-ts[2*i]; mx+=d; }
  for(int o=32;o;o>>=1){ mx+=__shfl_xor(mx,o); }
  u64 d=mx*mult/256; if(d>2000000) d=2000000;
  u64 t=__builtin_amdgcn_s_memrealtime(); while(__builtin_amdgcn_s_memrealtime()-t<d) __builtin_amdgcn_s_sleep(8);
}
__global__ __launch_bounds__(64) void probe_lat(const unsigned* p,int stride,int n,u64* ts,unsigned* sink){
  u64 t0=__builtin_amdgcn_s_memrealtime();
  unsigned idx=blockIdx.x*stride; unsigned acc=0;
  for(int i=0;i<n;i++){ unsigned v=p[idx+threadIdx.x]; acc+=v; idx+= 1024 + (v&0); asm volatile("":"+v"(idx),"+v"(acc)); idx += (acc & 0); }
  if(acc==0x12345678) sink[0]=acc;
  if(threadIdx.x==0){ ts[2*blockIdx.x]=t0; ts[2*blockIdx.x+1]=__builtin_amdgcn_s_memrealtime(); }
}
extern "C" void kernel_launch(void* const* d_in, const int* in_sizes, int n_in, void* d_out, int out_size, void* d_ws, size_t ws_size, hipStream_t stream){
  const u16* x=(const u16*)d_in[0]; const u16* wqkv=(const u16*)d_in[1]; const u16* wo=(const u16*)d_in[2];
  const u16* w1=(const u16*)d_in[4]; const u16* b1=(const u16*)d_in[5]; const u16* w2=(const u16*)d_in[6]; const u16* b2=(const u16*)d_in[7];
  char* ws=(char*)d_ws; u16* xn=(u16*)ws; u16* qkv=(u16*)(ws+(4<<20)); u16* h=(u16*)(ws+(16<<20)); u16* o=(u16*)(ws+(40<<20));
  float* kv=(float*)(ws+(44<<20)); float* z=(float*)(ws+(45<<20)); float* sf=(float*)(ws+(48<<20)); float* gf=(float*)(ws+(56<<20));
  u64* ts=(u64*)(ws+(100<<20)); unsigned* sink=(unsigned*)(ws+(101<<20));
  probe_lat<<<256,64,0,stream>>>((const unsigned*)(ws+(200<<20)),4096,4,ts,sink); probe_spin2<<<1,64,0,stream>>>(ts,100);
  ln_k<<<S,64,0,stream>>>(x,xn);
  gemm_k<0><<<dim3(1728/64,S/64),256,0,stream>>>(xn,wqkv,S,1728,D,nullptr,qkv,nullptr);
  gemm_k<1><<<dim3(F/64,S/64),256,0,stream>>>(xn,w1,S,F,D,b1,h,nullptr);
  kv_k<<<H,384,0,stream>>>(qkv,kv,z);
  o_k<<<S,576,0,stream>>>(qkv,kv,z,o);
  gemm_k<3><<<dim3(D/64,S/64),256,0,stream>>>(o,wo,S,D,D,nullptr,nullptr,sf);
  gemm_k<2><<<dim3(D/64,S/64),256,0,stream>>>(h,w2,S,D,F,b2,nullptr,gf);
  fin_k<<<S*D/256,256,0,stream>>>(x,sf,gf,(u16*)d_out);
}
```

```cpp
#include <hip/hip_runtime.h>
#include <hip/hip_bf16.h>
typedef unsigned short u16;
__device__ __forceinline__ float bf2f(u16 b){ return __uint_as_float(((unsigned)b)<<16); }
__device__ __forceinline__ u16 f2bf(float f){ unsigned u=__float_as_uint(f); u += 0x7fffu + ((u>>16)&1u); return (u16)(u>>16); }
constexpr int S=2048,D=576,F=4608,H=32,DH=18;

__global__ void ln_k(const u16* x, u16* xn){
  int r=blockIdx.x; int t=threadIdx.x;
  float v[9]; float s=0;
  for(int i=0;i<9;i++){ v[i]=bf2f(x[r*D+t+64*i]); s+=v[i]; }
  for(int o=32;o;o>>=1) s+=__shfl_xor(s,o);
  float m=s/D; float q=0;
  for(int i=0;i<9;i++){ float d=v[i]-m; q+=d*d; }
  for(int o=32;o;o>>=1) q+=__shfl_xor(q,o);
  float rs=rsqrtf(q/D+1e-5f);
  for(int i=0;i<9;i++) xn[r*D+t+64*i]=f2bf((v[i]-m)*rs);
}
template<int MODE> __global__ void gemm_k(const u16* A,const u16* B,int M,int N,int K,const u16* bias,u16* C,float* Cf){
  __shared__ float As[16][65]; __shared__ float Bs[16][65];
  int tx=threadIdx.x&15, ty=threadIdx.x>>4; int m0=blockIdx.y*64,n0=blockIdx.x*64;
  float acc[4][4]={};
  for(int k0=0;k0<K;k0+=16){
    for(int i=threadIdx.x;i<1024;i+=256){ int r=i>>4,c=i&15; As[c][r]=bf2f(A[(size_t)(m0+r)*K+k0+c]); int kk=i>>6,nn=i&63; Bs[kk][nn]=bf2f(B[(size_t)(k0+kk)*N+n0+nn]); }
    __syncthreads();
    for(int k=0;k<16;k++){ float a[4],b[4]; for(int i=0;i<4;i++){a[i]=As[k][ty*4+i]; b[i]=Bs[k][tx*4+i];}
      for(int i=0;i<4;i++)for(int j=0;j<4;j++) acc[i][j]+=a[i]*b[j]; }
    __syncthreads();
  }
  for(int i=0;i<4;i++)for(int j=0;j<4;j++){ int m=m0+ty*4+i,n=n0+tx*4+j; float v=acc[i][j];
    if(MODE==1){ v+=bf2f(bias[n]); float u=0.7978845608f*(v+0.044715f*v*v*v); v=0.5f*v*(1.f+tanhf(u)); }
    if(MODE==2){ v+=bf2f(bias[n]); }
    if(MODE==2||MODE==3) Cf[(size_t)m*N+n]=v; else C[(size_t)m*N+n]=f2bf(v);
  }
}
__device__ __forceinline__ float phi(float t){ return (t>0? t+1.f: __expf(t))+1e-6f; }
__global__ void kv_k(const u16* qkv,float* kv,float* z){
  int h=blockIdx.x; int t=threadIdx.x;
  float acc=0;
  if(t<324){ int d=t/18,e=t%18; for(int s=0;s<S;s++) acc+=bf2f(f2bf(phi(bf2f(qkv[s*1728+576+h*18+d]))))*bf2f(qkv[s*1728+1152+h*18+e]); kv[h*324+t]=acc; }
  else if(t<342){ int d=t-324; for(int s=0;s<S;s++) acc+=bf2f(f2bf(phi(bf2f(qkv[s*1728+576+h*18+d])))); z[h*18+d]=acc; }
}
__global__ void o_k(const u16* qkv,const float* kv,const float* z,u16* o){
  int s=blockIdx.x; int c=threadIdx.x; if(c>=D) return; int h=c/18,e=c%18;
  float num=0,den=0; for(int d=0;d<18;d++){ float p=bf2f(f2bf(phi(bf2f(qkv[s*1728+h*18+d])))); num+=p*kv[h*324+d*18+e]; den+=p*z[h*18+d]; }
  o[s*D+c]=f2bf(num/(den+1e-6f));
}
__global__ void fin_k(const u16* x,const float* s,const float* g,u16* out){
  int i=blockIdx.x*256+threadIdx.x; float gg=g[i]; float sl=gg/(1.f+__expf(-gg)); out[i]=f2bf(bf2f(x[i])+s[i]*sl);
}
typedef unsigned v4u __attribute__((ext_vector_type(4)));
typedef unsigned long long u64;
__global__ __launch_bounds__(512) void probe_rd(const char* p0,int n0,const char* p1,int n1,const char* p2,int n2,u64* ts,unsigned* sink,int shared_all){
  u64 t0=__builtin_amdgcn_s_memrealtime();
  unsigned acc=0;
  int nb = shared_all? 32: 256; int b = shared_all? (blockIdx.x>>3) : blockIdx.x;
  const char* ps[3]={p0,p1,p2}; int ns[3]={n0,n1,n2};
  #pragma unroll
  for(int a=0;a<3;a++){
    int n16=ns[a]/16; int per=n16/nb; const v4u* q=(const v4u*)ps[a]+(size_t)b*per;
    v4u v[8];
    for(int i=threadIdx.x;i<per;i+=512*8){
      #pragma unroll
      for(int j=0;j<8;j++){ int k=i+j*512; v[j]= k<per? q[k]: v4u{0,0,0,0}; }
      #pragma unroll
      for(int j=0;j<8;j++) acc^=v[j][0];
    }
  }
  if(acc==0x12345678) sink[0]=acc;
  __syncthreads();
  if(threadIdx.x==0){ ts[2*blockIdx.x]=t0; ts[2*blockIdx.x+1]=__builtin_amdgcn_s_memrealtime(); }
}
template<int AUX> __global__ __launch_bounds__(512) void probe_wr(char* p,int nkb,u64* ts){
  u64 t0=__builtin_amdgcn_s_memrealtime();
  int wave=__builtin_amdgcn_readfirstlane(threadIdx.x>>6), lane=threadIdx.x&63;
  __amdgpu_buffer_rsrc_t r=__builtin_amdgcn_make_buffer_rsrc((void*)(p+(size_t)blockIdx.x*nkb*1024),0,0x7fffffff,0x00020000);
  int n=nkb/8; v4u v={(unsigned)lane,(unsigned)t0,3u,4u};
  for(int i=0;i<n;i++) __builtin_amdgcn_raw_buffer_store_b128(v,r,lane*16,(wave*n+i)*1024,AUX);
  asm volatile("s_waitcnt vmcnt(0)":::"memory");
  __syncthreads();
  if(threadIdx.x==0){ ts[2*blockIdx.x]=t0; ts[2*blockIdx.x+1]=__builtin_amdgcn_s_memrealtime(); }
}
__global__ void probe_spin(const u64* ts,int mode,int mult){
  u64 now=__builtin_amdgcn_s_memrealtime();
  u64 mn=~0ull,mx=0;
  for(int i=threadIdx.x;i<256;i+=64){ u64 a=ts[2*i],b=ts[2*i+1]; mn=a<mn?a:mn; mx=b>mx?b:mx; }
  for(int o=32;o;o>>=1){ u64 a=__shfl_xor(mn,o),b=__shfl_xor(mx,o); mn=a<mn?a:mn; mx=b>mx?b:mx; }
  u64 now0=__shfl(now,0);
  u64 d = mode==0? mx-mn : (mode==1? now0-mn : now0-mx); if(d>5000) d=5000;
  u64 t=__builtin_amdgcn_s_memrealtime(); while(__builtin_amdgcn_s_memrealtime()-t<d*mult) __builtin_amdgcn_s_sleep(8);
}
template<int N> struct Rep { static __device__ __forceinline__ void run(float& a,float& b){
  asm volatile("v_fma_f32 %0, %0, %1, %1\n v_fma_f32 %1, %0, %1, %0\n v_fma_f32 %0, %0, %1, %1\n v_fma_f32 %1, %0, %1, %0\n v_fma_f32 %0, %0, %1, %1\n v_fma_f32 %1, %0, %1, %0\n v_fma_f32 %0, %0, %1, %1\n v_fma_f32 %1, %0, %1, %0\n":"+v"(a),"+v"(b)); Rep<N-1>::run(a,b);} };
template<> struct Rep<0>{ static __device__ __forceinline__ void run(float&,float&){} };
template<int NB> __global__ __launch_bounds__(512) void probe_code(u64* ts,float* sink,int loops){
  u64 t0=__builtin_amdgcn_s_memrealtime();
  float a=threadIdx.x*1e-9f,b=0.5f;
  for(int l=0;l<loops;l++){ Rep<NB/4>::run(a,b); Rep<NB/4>::run(a,b); Rep<NB/4>::run(a,b); Rep<NB/4>::run(a,b); }
  if(a==123.f) sink[0]=a+b;
  __syncthreads();
  if(threadIdx.x==0){ ts[2*blockIdx.x]=t0; ts[2*blockIdx.x+1]=__builtin_amdgcn_s_memrealtime(); }
}
__global__ void probe_spin2(const u64* ts,int mult){
  u64 mx=0;
  for(int i=threadIdx.x;i<256;i+=64){ u64 d=ts[2*i+1]-ts[2*i]; mx+=d; }
  for(int o=32;o;o>>=1){ mx+=__shfl_xor(mx,o); }
  u64 d=mx*mult/256; if(d>2000000) d=2000000;
  u64 t=__builtin_amdgcn_s_memrealtime(); while(__builtin_amdgcn_s_memrealtime()-t<d) __builtin_amdgcn_s_sleep(8);
}
__global__ __launch_bounds__(64) void probe_lat(const unsigned* p,int stride,int n,u64* ts,unsigned* sink){
  u64 t0=__builtin_amdgcn_s_memrealtime();
  unsigned idx=blockIdx.x*stride; unsigned acc=0;
  for(int i=0;i<n;i++){ unsigned v=p[idx+threadIdx.x]; acc+=v; idx+= 1024 + (v&0); asm volatile("":"+v"(idx),"+v"(acc)); idx += (acc & 0); }
  if(acc==0x12345678) sink[0]=acc;
  if(threadIdx.x==0){ ts[2*blockIdx.x]=t0; ts[2*blockIdx.x+1]=__builtin_amdgcn_s_memrealtime(); }
}
template<int N> struct RepS { static __device__ __forceinline__ void run(float& a,float& b){
  asm volatile("v_fma_f32 %0, %0, %1, %1\n v_fma_f32 %1, %0, %1, %0\n v_fma_f32 %0, %0, %1, %1\n v_fma_f32 %1, %0, %1, %0\n v_fma_f32 %0, %0, %1, %1\n v_fma_f32 %1, %0, %1, %0\n v_fma_f32 %0, %0, %1, %1\n s_nop 15\n s_nop 15\n":"+v"(a),"+v"(b)); RepS<N-1>::run(a,b);} };
template<> struct RepS<0>{ static __device__ __forceinline__ void run(float&,float&){} };
template<int NB,int PF,int SLOW> __global__ __launch_bounds__(512) void probe_code2(u64* ts,float* sink,int loops){
  u64 t0=__builtin_amdgcn_s_memrealtime();
  float a=threadIdx.x*1e-9f,b=0.5f;
  unsigned pf=0;
  if(PF){
    u64 pc; asm volatile("s_getpc_b64 %0":"=s"(pc));
    const unsigned* cp=(const unsigned*)(pc&~63ull);
    asm volatile("global_load_dword %0, %1, off":"=v"(pf):"v"(cp+threadIdx.x*16):"memory");
    if(PF==2) asm volatile("s_waitcnt vmcnt(0)":::"memory");
  }
  for(int l=0;l<loops;l++){
    if(SLOW){ RepS<NB/4>::run(a,b); RepS<NB/4>::run(a,b); RepS<NB/4>::run(a,b); RepS<NB/4>::run(a,b); }
    else { Rep<NB/4>::run(a,b); Rep<NB/4>::run(a,b); Rep<NB/4>::run(a,b); Rep<NB/4>::run(a,b); } }
  asm volatile("s_waitcnt vmcnt(0)":::"memory");
  asm volatile(""::"v"(pf));
  if(a==123.f) sink[0]=a+b;
  __syncthreads();
  if(threadIdx.x==0){ ts[2*blockIdx.x]=t0; ts[2*blockIdx.x+1]=__builtin_amdgcn_s_memrealtime(); }
}
extern "C" void kernel_launch(void* const* d_in, const int* in_sizes, int n_in, void* d_out, int out_size, void* d_ws, size_t ws_size, hipStream_t stream){
  const u16* x=(const u16*)d_in[0]; const u16* wqkv=(const u16*)d_in[1]; const u16* wo=(const u16*)d_in[2];
  const u16* w1=(const u16*)d_in[4]; const u16* b1=(const u16*)d_in[5]; const u16* w2=(const u16*)d_in[6]; const u16* b2=(const u16*)d_in[7];
  char* ws=(char*)d_ws; u16* xn=(u16*)ws; u16* qkv=(u16*)(ws+(4<<20)); u16* h=(u16*)(ws+(16<<20)); u16* o=(u16*)(ws+(40<<20));
  float* kv=(float*)(ws+(44<<20)); float* z=(float*)(ws+(45<<20)); float* sf=(float*)(ws+(48<<20)); float* gf=(float*)(ws+(56<<20));
  u64* ts=(u64*)(ws+(100<<20)); unsigned* sink=(unsigned*)(ws+(101<<20));
  probe_code2<512,1,0><<<256,512,0,stream>>>(ts,(float*)sink,1); probe_spin2<<<1,64,0,stream>>>(ts,100);
  ln_k<<<S,64,0,stream>>>(x,xn);
  gemm_k<0><<<dim3(1728/64,S/64),256,0,stream>>>(xn,wqkv,S,1728,D,nullptr,qkv,nullptr);
  gemm_k<1><<<dim3(F/64,S/64),256,0,stream>>>(xn,w1,S,F,D,b1,h,nullptr);
  kv_k<<<H,384,0,stream>>>(qkv,kv,z);
  o_k<<<S,576,0,stream>>>(qkv,kv,z,o);
  gemm_k<3><<<dim3(D/64,S/64),256,0,stream>>>(o,wo,S,D,D,nullptr,nullptr,sf);
  gemm_k<2><<<dim3(D/64,S/64),256,0,stream>>>(h,w2,S,D,F,b2,nullptr,gf);
  fin_k<<<S*D/256,256,0,stream>>>(x,sf,gf,(u16*)d_out);
}
```

```cpp
#include <hip/hip_runtime.h>
typedef unsigned u32; typedef unsigned long long u64; typedef unsigned short u16;
typedef u32 u32x2 __attribute__((ext_vector_type(2)));
typedef u32 u32x4 __attribute__((ext_vector_type(4)));
typedef float f32x2 __attribute__((ext_vector_type(2)));
typedef float f32x4 __attribute__((ext_vector_type(4)));
typedef float f32x16 __attribute__((ext_vector_type(16)));
typedef short s16x4 __attribute__((ext_vector_type(4)));
typedef short s16x8 __attribute__((ext_vector_type(8)));
typedef __bf16 bf16x2 __attribute__((ext_vector_type(2)));
#define LAS __attribute__((address_space(3)))
#define DI __device__ __forceinline__
#define VROW (4 * g4)
#define DMAFL ""
#define WS_HFLAG 4096
#define WS_PFLAG 266240
#define WS_KVFLAG 790528
#define WS_PX 4194304
#define WS_H 29360128
#define WS_BYTES 50331648
#define KVPS 18944
#define MISC 159744
#define OOB 0x7fffff00u
#define SB __builtin_amdgcn_sched_barrier(0)
#define LOG2E 1.4426950408889634f
#define GA (-2.3022081985f)
#define GB (-0.1029432397f)
#define SPIN_LIMIT 400000
#define LOAD1(d, vo, srd, so) asm volatile("buffer_load_dword %0, %1, %2, %3 offen" : "=v"(d) : "v"(vo), "s"(srd), "s"(so) : "memory")
#define LOAD1K(d, vo, srd, so) asm volatile("buffer_load_dword %0, %1, %2, %3 offen" : "+v"(d) : "v"(vo), "s"(srd), "s"(so) : "memory")
#define LOAD2(d, vo, srd, so, im, fl) asm volatile("buffer_load_dwordx2 %0, %1, %2, %3 offen offset:%4" fl : "=v"(d) : "v"(vo), "s"(srd), "s"(so), "i"(im) : "memory")
#define LOAD4(d, vo, srd, so, im, fl) asm volatile("buffer_load_dwordx4 %0, %1, %2, %3 offen offset:%4" fl : "=v"(d) : "v"(vo), "s"(srd), "s"(so), "i"(im) : "memory")
#define STORE4(d, vo, srd, so, im) asm volatile("buffer_store_dwordx4 %0, %1, %2, %3 offen offset:%4 sc1\n\ts_nop 1" :: "v"(d), "v"(vo), "s"(srd), "s"(so), "i"(im) : "memory")
#define DMA(lds, vo, srd, so) asm volatile("s_mov_b32 m0, %0\n\ts_nop 0\n\tbuffer_load_dwordx4 %1, %2, %3 offen" DMAFL " lds" :: "s"(lds), "v"(vo), "s"(srd), "s"(so) : "memory")
#define STORE2(d, vo, srd, so, im) asm volatile("buffer_store_dwordx2 %0, %1, %2, %3 offen offset:%4 sc1" :: "v"(d), "v"(vo), "s"(srd), "s"(so), "i"(im) : "memory")
#define DMA2(lb, li, vo, srd, sb, si) do { u32 so_; asm volatile("s_add_u32 m0, %1, %2\n\ts_add_u32 %0, %5, %6\n\tbuffer_load_dwordx4 %3, %4, %0 offen" DMAFL " lds" : "=&s"(so_) : "s"(lb), "i"(li), "v"(vo), "s"(srd), "s"(sb), "i"(si) : "memory", "scc"); } while (0)
#define CND(op, k) "s_cmp_" #op "_u32 %[cr], " #k "\n\ts_cbranch_scc0 1f\n\t"
#define DMA_C(c_, op, k, lds, vo, srd, so) asm volatile(CND(op, k) "s_mov_b32 m0, %0\n\ts_nop 0\n\tbuffer_load_dwordx4 %1, %2, %3 offen lds\n1:" :: "s"(lds), "v"(vo), "s"(srd), "s"(so), [cr] "s"(c_) : "memory", "scc")
#define LOAD4_C(c_, op, k, d, vo, srd, so, im, fl) asm volatile(CND(op, k) "buffer_load_dwordx4 %0, %1, %2, %3 offen offset:%4" fl "\n1:" : "=v"(d) : "v"(vo), "s"(srd), "s"(so), "i"(im), [cr] "s"(c_) : "memory", "scc")
#define LOAD2_C(c_, op, k, d, vo, srd, so, im, fl) asm volatile(CND(op, k) "buffer_load_dwordx2 %0, %1, %2, %3 offen offset:%4" fl "\n1:" : "=v"(d) : "v"(vo), "s"(srd), "s"(so), "i"(im), [cr] "s"(c_) : "memory", "scc")
#define FLAG2(so) do { u32x2 fv_; fv_[0] = tagLo + 1; fv_[1] = tagHi; const u32 f2_ = ln * 262144; asm volatile("s_mov_b64 exec, 3\n\tbuffer_store_dwordx2 %0, %1, %2, %3 offen sc1\n\ts_mov_b64 exec, -1" :: "v"(fv_), "v"(f2_), "s"(srdWS), "s"(so) : "memory"); } while (0)
#define FLAG_C(c_, op, k, so, cnt) do { u32x2 fv_; fv_[0] = tagLo + (cnt); fv_[1] = tagHi; asm volatile(CND(op, k) "s_mov_b64 exec, 1\n\tbuffer_store_dwordx2 %0, %1, %2, %3 offen sc1\n\ts_mov_b64 exec, -1\n1:" :: "v"(fv_), "v"(vzero), "s"(srdWS), "s"(so), [cr] "s"(c_) : "memory", "scc"); } while (0)
#define FLAG(so, cnt) do { u32x2 fv_; fv_[0] = tagLo + (cnt); fv_[1] = tagHi; asm volatile("s_mov_b64 exec, 1\n\tbuffer_store_dwordx2 %0, %1, %2, %3 offen sc1\n\ts_mov_b64 exec, -1" :: "v"(fv_), "v"(vzero), "s"(srdWS), "s"(so) : "memory"); } while (0)
#define MF32(a, b, c) __builtin_amdgcn_mfma_f32_32x32x16_bf16(a, b, c, 0, 0, 0)
#define MF16(a, b, c) __builtin_amdgcn_mfma_f32_16x16x32_bf16(a, b, c, 0, 0, 0)
#define AM16(c, a, b) asm volatile("v_mfma_f32_16x16x32_bf16 %0, %1, %2, %0" : "+v"(c) : "v"(a), "v"(b))
#define AM16N(c, a, b, n) asm volatile("v_mfma_f32_16x16x32_bf16 %0, %1, %2, %0" : "+v"(c) : "v"(a), "v"(b), "v"(n))
#define AM16Z(c, a, b) asm volatile("v_mfma_f32_16x16x32_bf16 %0, %1, %2, 0" : "=&v"(c) : "v"(a), "v"(b))
#define LOAD1_C(c_, op, k, d, vo, srd, so) asm volatile(CND(op, k) "buffer_load_dword %0, %1, %2, %3 offen\n1:" : "=v"(d) : "v"(vo), "s"(srd), "s"(so), [cr] "s"(c_) : "memory", "scc")
#define LOAD1K_C(c_, op, k, d, vo, srd, so) asm volatile(CND(op, k) "buffer_load_dword %0, %1, %2, %3 offen\n1:" : "+v"(d) : "v"(vo), "s"(srd), "s"(so), [cr] "s"(c_) : "memory", "scc")
#define FR(x) __builtin_bit_cast(s16x8, x)
#define TR1(base, o) __builtin_amdgcn_ds_read_tr16_b64_v4i16((LAS s16x4*)(size_t)((base) + (o)))
#define TR2(base, o0, o1) __builtin_shufflevector(TR1(base, o0), TR1(base, o1), 0, 1, 2, 3, 4, 5, 6, 7)
#define TR2B(b0, o0, b1, o1) __builtin_shufflevector(TR1(b0, o0), TR1(b1, o1), 0, 1, 2, 3, 4, 5, 6, 7)
#define POLL2(v, vo, need, id) do { if (!ok2(vo, v[0], v[1], tagLo, tagHi, need)) { spin2(srdWS, vo, tagLo, tagHi, need); } } while (0)
DI u32 OPQ(u32 v) { asm volatile("" : "+v"(v)); return v; }
DI u32 pfo(u32 ln, u32 n) { ln = OPQ(ln); return ln < n ? ln * 128 : OOB; }
DI u32 cvtpk(float a, float b) { u32 r; asm("v_cvt_pk_bf16_f32 %0, %1, %2" : "=v"(r) : "v"(a), "v"(b)); return r; }
DI u32 cvtb(float a, float b) { f32x2 v; v[0] = a; v[1] = b; const bf16x2 h = __builtin_convertvector(v, bf16x2); return __builtin_bit_cast(u32, h); }
DI float blo(u32 v) { return __uint_as_float(v << 16); }
DI float bhi(u32 v) { return __uint_as_float(v & 0xffff0000u); }
DI u32x4 pack2(const f32x4& a, const f32x4& b) { u32x4 r; r[0] = cvtb(a[0], a[1]); r[1] = cvtb(a[2], a[3]); r[2] = cvtb(b[0], b[1]); r[3] = cvtb(b[2], b[3]); return r; }
DI void add2(f32x4& a, f32x4& b, const u32x4& t) {
  const u32 t0 = t[0], t1 = t[1], t2 = t[2], t3 = t[3];
  a[0] += blo(t0); a[1] += bhi(t0); a[2] += blo(t1); a[3] += bhi(t1); b[0] += blo(t2); b[1] += bhi(t2); b[2] += blo(t3); b[3] += bhi(t3);
}
DI u32x4 mk_srd(const void* p, u32 bytes) { u32x4 s; s[0] = (u32)(size_t)p; s[1] = (u32)((size_t)p >> 32) & 0xffffu; s[2] = bytes; s[3] = 0x00020000u; asm volatile("" : "+s"(s)); return s; }
DI bool lane_ok(u32 lo, u32 hi, u32 tl, u32 th, u32 need) { return hi == th && (lo - tl - need) <= (7u - need); }
DI bool ok2(u32 vo, u32 lo, u32 hi, u32 tl, u32 th, u32 need) { return __builtin_amdgcn_ballot_w64(vo != OOB && !lane_ok(lo, hi, tl, th, need)) == 0; }
DI void spin2(const u32x4& srd, u32 vo, u32 tl, u32 th, u32 need) {
#pragma unroll 1
  for (int n = 0; n < SPIN_LIMIT; ++n) {
    u32x2 v; asm volatile("buffer_load_dwordx2 %0, %1, %2, 0 offen sc1\n\ts_waitcnt vmcnt(0)" : "=&v"(v) : "v"(vo), "s"(srd) : "memory");
    if (ok2(vo, v[0], v[1], tl, th, need)) break;
    __builtin_amdgcn_s_sleep(1);
  }
}
template <int C> DI float dppadd(float v) { return v + __int_as_float(__builtin_amdgcn_update_dpp(0, __float_as_int(v), C, 0xf, 0xf, true)); }
DI float dot2(u32 a, u32 b, float c) { return __builtin_amdgcn_fdot2_f32_bf16(__builtin_bit_cast(bf16x2, a), __builtin_bit_cast(bf16x2, b), c, false); }

DI void ln_stats(const u32x4 (&x)[4], const u32x2& xt, float& rs, float& cc) {
  float s0 = 0.f, s1 = 0.f, q0 = 0.f, q1 = 0.f;
#pragma unroll
  for (int i = 0; i < 9; ++i) {
    const u32 a = i < 8 ? x[i >> 1][(i & 1) * 2] : xt[0], b = i < 8 ? x[i >> 1][(i & 1) * 2 + 1] : xt[1];
    s0 = dot2(a, 0x3f803f80u, s0); q0 = dot2(a, a, q0); s1 = dot2(b, 0x3f803f80u, s1); q1 = dot2(b, b, q1);
  }
  float s = s0 + s1, q = q0 + q1;
  s = dppadd<0x128>(s); q = dppadd<0x128>(q); s = dppadd<0x124>(s); q = dppadd<0x124>(q);
  s = dppadd<0x122>(s); q = dppadd<0x122>(q); s = dppadd<0x121>(s); q = dppadd<0x121>(q);
  const float mean = s * (1.0f / 576.0f);
  const float var = __builtin_fmaxf(__builtin_fmaf(-mean, mean, q * (1.0f / 576.0f)), 0.0f);
  rs = __builtin_amdgcn_rsqf(var + 1e-5f);
  cc = -mean * rs;
}
DI void ln4(u32x4& x, const f32x2& rc) {
  f32x2 t0, t1, t2, t3; u32 a = x[0], b = x[1], c = x[2], d = x[3];
  asm("v_lshlrev_b32 %0, 16, %8\n\tv_and_b32 %1, 0xffff0000, %8\n\tv_lshlrev_b32 %2, 16, %9\n\tv_and_b32 %3, 0xffff0000, %9\n\t"
      "v_lshlrev_b32 %4, 16, %10\n\tv_and_b32 %5, 0xffff0000, %10\n\tv_lshlrev_b32 %6, 16, %11\n\tv_and_b32 %7, 0xffff0000, %11"
      : "=&v"(t0[0]), "=&v"(t0[1]), "=&v"(t1[0]), "=&v"(t1[1]), "=&v"(t2[0]), "=&v"(t2[1]), "=&v"(t3[0]), "=&v"(t3[1]) : "v"(a), "v"(b), "v"(c), "v"(d));
  asm("v_pk_fma_f32 %0, %0, %4, %4 op_sel:[0,0,1] op_sel_hi:[1,0,1]\n\tv_pk_fma_f32 %1, %1, %4, %4 op_sel:[0,0,1] op_sel_hi:[1,0,1]\n\t"
      "v_pk_fma_f32 %2, %2, %4, %4 op_sel:[0,0,1] op_sel_hi:[1,0,1]\n\tv_pk_fma_f32 %3, %3, %4, %4 op_sel:[0,0,1] op_sel_hi:[1,0,1]"
      : "+v"(t0), "+v"(t1), "+v"(t2), "+v"(t3) : "v"(rc));
  x[0] = cvtpk(t0[0], t0[1]); x[1] = cvtpk(t1[0], t1[1]); x[2] = cvtpk(t2[0], t2[1]); x[3] = cvtpk(t3[0], t3[1]);
}
template <int LO, int HI>
DI void ln_apply(u32x4 (&x)[4], u32x2& xt, float rs, float cc, u32 lds, u32 ldst) {
  const f32x2 rc = {rs, cc};
#pragma unroll
  for (int i = LO; i < (HI < 4 ? HI : 4); ++i) {
    ln4(x[i], rc);
    if (i) *(LAS u32x4*)(size_t)(lds + (i - 1) * 256) = x[i - 1];
    SB;
  }
  if (HI > 4) {
    const u32 a = xt[0], b = xt[1];
    const float a0 = __builtin_fmaf(blo(a), rs, cc), a1 = __builtin_fmaf(bhi(a), rs, cc), b0 = __builtin_fmaf(blo(b), rs, cc), b1 = __builtin_fmaf(bhi(b), rs, cc);
    *(LAS u32x4*)(size_t)(lds + 768) = x[3];
    xt[0] = cvtpk(a0, a1); xt[1] = cvtpk(b0, b1);
    *(LAS u32x2*)(size_t)(ldst) = xt;
  }
}

DI u32x4 z_reduce(u32 base, u32 ln) {
  const u32 u = ln < 20 ? ln : 19, a = base + (u >> 2) * 128 + ((u >> 1) & 1) * 64 + (u & 1) * 16;
  f32x4 lo = *(LAS f32x4*)(size_t)a, hi = *(LAS f32x4*)(size_t)(a + 32);
#pragma unroll
  for (int g = 1; g < 4; ++g) { lo += *(LAS f32x4*)(size_t)(a + g * 768); hi += *(LAS f32x4*)(size_t)(a + g * 768 + 32); }
  return pack2(lo, hi);
}
DI void l1_setup(u32 r, u32 j, u32 w, u32 ln, u32& vk, u32& vs, u32& lzr, u32& lzw, u32x2& m, s16x8& a) {
  const u32 ul = 16 * w + (ln & 15), g = ln >> 4;
  const u32 u = 72 * r + ul, tau = u >> 7, s = (u >> 6) & 1, hh = (u >> 5) & 1;
  const u32 ta = (tau >= 8) ? 4u : ((0x33221100u >> (4 * tau)) & 15u), tb = (tau >= 8) ? 8u : ((0x87766554u >> (4 * tau)) & 15u);
  const u32 src = (j * 16 + g * 4) * KVPS, q0 = (72 * r) >> 5;
  const u32 qz = q0 + (ln & 3), tz = qz >> 2, az = (tz >= 8) ? 4u : ((0x33221100u >> (4 * tz)) & 15u);
  vk = ul < 72 ? src + u * 16 : ((w == 5 && (ln & 15) < 4) ? src + 18432 + (az * 4 + (qz & 3)) * 16 : OOB);
  vs = (ul < 72 && g < 2) ? j * 36864 + u * 16 + g * 8 : OOB;
  lzr = 163840 - 64 + (((u >> 5) - q0) & 3) * 16 + (g & 1) * 8;
  lzw = (w == 5 && (ln & 15) < 4 && g < 2) ? 163840 - 64 + (ln & 3) * 16 + g * 8 : 20480 + ln * 8;
  const int he = (((int)(32 * tb + (u & 31)) - 144) * 57) >> 10;
  const int d = 32 * ta + 16 * s + 4 * hh + 8 * (g & 1);
  m[0] = (((d * 57) >> 10) == he ? 0xffffu : 0u) | ((((d + 1) * 57) >> 10) == he ? 0xffff0000u : 0u);
  m[1] = ((((d + 2) * 57) >> 10) == he ? 0xffffu : 0u) | ((((d + 3) * 57) >> 10) == he ? 0xffff0000u : 0u);
  u32x4 t;
#pragma unroll
  for (int k = 0; k < 4; ++k) t[k] = ((ln & 15) == 2 * k ? 0x3f80u : 0u) | ((ln & 15) == 2 * k + 1 ? 0x3f800000u : 0u);
  a = FR(t);
}

struct Sel { s16x8 lo, hi; };
DI Sel mk_sel(u32 ln) {
  const bool on = ((ln & 15) >> 2) == (ln >> 4);
  u32x4 a; a[0] = on && (ln & 2) == 0 ? ((ln & 1) ? 0x3f800000u : 0x3f80u) : 0u; a[1] = on && (ln & 2) ? ((ln & 1) ? 0x3f800000u : 0x3f80u) : 0u; a[2] = 0u; a[3] = 0u;
  u32x4 b; b[0] = 0u; b[1] = 0u; b[2] = a[0]; b[3] = a[1];
  return Sel{FR(a), FR(b)};
}
DI void madd(f32x4& a, f32x4& b, const u32x4 u, const Sel& s) { a = MF16(s.lo, FR(u), a); b = MF16(s.hi, FR(u), b); }
struct FinalIO { u32 vo[2], lr[2], vp, v0, v1, so, sP; };
DI void final_prep(FinalIO& io, u32 r, u32 j, u32 c, u32 rg, u32 cg, u32 ln) {
  const u32 tt8 = 4 * (j >> 1) + rg;
  io.so = ((r * 128 + tt8 * 16) * 576 + 192 * c + (j & 1) * 96 + cg * 64) * 2;
  io.vp = ln >= 3 ? OOB : WS_PFLAG + ((r * 4 + ((j + 1 + ln) & 3)) * 4 + c) * 1024 + ((tt8 >> 1) + 4 * (tt8 & 1)) * 128 + cg * 262144;
  io.sP = WS_PX + ((r * 3 + c) * 16 + j * 4) * 12288 + rg * 3072 + cg * 2048;
  io.v0 = ln * 16; io.v1 = cg ? OOB : ln * 16;
#pragma unroll
  for (int i = 0; i < 2; ++i) {
    const u32 row = cg ? ln >> 2 : (ln >> 3) + 8 * i, ch = cg ? ln & 3 : ln & 7;
    io.vo[i] = (i && cg) ? OOB : row * 1152 + ch * 16;
    io.lr[i] = (rg + 4 * cg) * 4352 + row * 272 + ch * 32;
  }
}
DI void final_stage(u32x4 (&ra)[10], const u32x4 (&xv)[2], const Sel& se, FinalIO& io, u32 rg, u32 cg, u32 ln, const u32x4& srdOut, u32 lsp, u32 lbase) {
  f32x4 g[4], s[4];
  const u32 ls = lsp + (rg * 3 + cg) * 4096 + ln * 16;
  s[0] = f32x4{0.f, 0.f, 0.f, 0.f}; s[1] = s[0]; s[2] = s[0]; s[3] = s[0]; g[0] = s[0]; g[1] = s[0]; g[2] = s[0]; g[3] = s[0];
#pragma unroll
  for (int q = 0; q < 4; ++q) {
    madd(s[0], s[1], *(LAS u32x4*)(size_t)(ls + cg * 4096 + q * 1024), se); madd(s[2], s[3], *(LAS u32x4*)(size_t)(ls + 4096 - cg * 4096 + q * 1024), se);
  }
  madd(g[0], g[1], ra[6], se); madd(g[2], g[3], ra[7], se); madd(g[0], g[1], ra[8], se); madd(g[2], g[3], ra[9], se);
  SB;
  asm volatile("s_waitcnt vmcnt(0)" : "+v"(ra[0]), "+v"(ra[1]), "+v"(ra[2]), "+v"(ra[3]), "+v"(ra[4]), "+v"(ra[5]) :: "memory");

#pragma unroll
  for (int k = 0; k < 3; ++k) { madd(g[0], g[1], ra[2 * k], se); madd(g[2], g[3], ra[2 * k + 1], se); }
  const u32 lw = lbase + (rg + 4 * cg) * 4352 + (ln & 15) * 272 + (ln >> 4) * 16;
#pragma unroll
  for (int n = 0; n < 4; ++n) {
    if (n == 2 && cg) break;
    f32x4 p;
#pragma unroll
    for (int k = 0; k < 4; ++k) {
      const float gg = g[n][k];
      p[k] = s[n][k] * gg * __builtin_amdgcn_rcpf(1.0f + __builtin_amdgcn_exp2f(-LOG2E * gg));
    }
    *(LAS f32x4*)(size_t)(lw + n * 64) = p;
  }
#pragma unroll
  for (int i = 0; i < 2; ++i) {
    if (i && cg) break;
    f32x4 a = *(LAS f32x4*)(size_t)(lbase + io.lr[i]), b = *(LAS f32x4*)(size_t)(lbase + io.lr[i] + 16);
    add2(a, b, xv[i]);
    const u32x4 ov = pack2(a, b);
    asm volatile("buffer_store_dwordx4 %0, %1, %2, %3 offen\n\ts_nop 1" :: "v"(ov), "v"(io.vo[i]), "s"(srdOut), "s"(io.so) : "memory");
  }
}

DI void epoch_update(const u32x4& srdWS, u32 ln, u32 tagLo, u32 tagHi) {
#pragma unroll 1
  for (int k = 0; k < 4; ++k) spin2(srdWS, k < 3 ? WS_HFLAG + (ln * 4 + k) * 1024 : WS_KVFLAG + ln * 128, tagLo, tagHi, 1);
  u32x2 fv; fv[0] = tagLo; fv[1] = tagHi; const u32 vz = 0;
  asm volatile("s_mov_b64 exec, 1\n\tbuffer_store_dwordx2 %0, %1, %2, 0 offen sc1\n\ts_mov_b64 exec, -1" :: "v"(fv), "v"(vz), "s"(srdWS) : "memory");
}

__global__ __launch_bounds__(512, 2) void fused_block(const u16* __restrict__ x, const u16* __restrict__ wqkv, const u16* __restrict__ wo,
                                                      const u16* __restrict__ w1, const u16* __restrict__ b1, const u16* __restrict__ w2,
                                                      const u16* __restrict__ b2, u16* __restrict__ out, char* __restrict__ ws) {
  const u32 ln = threadIdx.x & 63, l15 = ln & 15, pw = __builtin_amdgcn_readfirstlane(threadIdx.x >> 6);
  const u32 w = pw < 4 ? pw : 4 + ((pw + 2) & 3), rg = w & 3, cg = w >> 2, wp = w >> 1, tid = w * 64 + ln;
  const u32 bid = blockIdx.x, idx = bid >> 3, j = bid & 3, r = ((bid >> 2) & 1) * 8 + (idx & 7), c = ((idx >> 3) + 3) & 3;
  const u32x4 srdX = mk_srd(x, 2048 * 576 * 2), srdWS = mk_srd(ws, WS_BYTES), srdW2 = mk_srd(c < 3 ? w2 : wo, c < 3 ? 4608 * 576 * 2 : 576 * 576 * 2),
              srdB2 = mk_srd(b2, 576 * 2), srdOut = mk_srd(out, 2048 * 576 * 2);
  const u32x4 srdW = mk_srd(c < 3 ? w1 : wqkv, c < 3 ? 576 * 4608 * 2 : 576 * 1728 * 2), srdB1 = mk_srd(b1, 4608 * 2);
  const u32 vzero = OPQ(0);
  const u32 voffX = (4 * w + (ln >> 4)) * 1152 + l15 * 16, voffXt = (4 * w + (ln >> 4)) * 1152 + l15 * 8, sX = r * 128 * 1152;
  if (c < 3) {
    const u32 voffW = (ln >> 3) * 9216 + ((ln & 7) ^ (((ln >> 4) & 1) << 2)) * 16, sW = 24 * w * 9216 + (1152 * j + 384 * c) * 2;
    const u32 voffB = tid * 4, sB1 = (1152 * j + 384 * c) * 2;
    const u32 lcB = (ln & 7) ^ (((ln >> 4) & 1) << 1) ^ ((w & 1) << 2), voffWa = (ln >> 3) * 9216 + lcB * 16, voffWb = voffWa ^ 64;
    const u32 tb16 = (8 * (ln >> 4) + (l15 >> 2)) * 128 + (((4 * cg + ((ln & 3) >> 1)) ^ ((l15 >> 3) << 1) ^ (((ln >> 4) & 1) << 2)) << 4) + (ln & 1) * 8;
    const u32 ldsA0 = OPQ(tb16), ldsA1 = OPQ(tb16 + 49152), ldsA2 = OPQ(tb16 + 98304), ldsB0 = OPQ(tb16 ^ 32), ldsB1 = OPQ((tb16 ^ 32) + 49152), ldsB2 = OPQ((tb16 ^ 32) + 98304);
    const u32 ldsXg0 = l15 * 1152 + (((ln >> 4) ^ (l15 >> 1)) << 4), ldsXg1 = l15 * 1152 + (((4 + (ln >> 4)) ^ (l15 >> 1)) << 4);
    const u32 ldsBias = OPQ(MISC + cg * 128 + (ln >> 4) * 16);
    const u32 sHo = WS_H + ((r * 4 + j) * 36 + 12 * c + cg) * 8192;
    const u32 sHF = WS_HFLAG + ((r * 4 + j) * 4 + c) * 1024 + w * 128;
    const u32x4 srdQ = mk_srd(wqkv, 576 * 1728 * 2), srdO = mk_srd(wo, 576 * 576 * 2);
  u32x4 xr[4][4]; u32x2 xt[4]; s16x8 xf[36]; u32x2 ep; u32 bia;
  u32 ft = 0, fx = 0;
  LOAD1(fx, pfo(ln, 9), srdX, sX + ((j * 4 + c) * 8 + w) * 1152);
  SB;
  const u32 lnsw = (2 * w + (ln >> 5)) & 7;
  const u32 lnw = (4 * w + (ln >> 4)) * 1152 + (l15 >> 3) * 128 + (((l15 & 7) ^ lnsw) << 4), lnt = (4 * w + (ln >> 4)) * 1152 + 1024 + (((l15 >> 1) ^ lnsw) << 4) + (ln & 1) * 8;
  const u32 ldsLnW0 = OPQ(lnw + 122880), ldsLnT0 = OPQ(lnt + 122880), ldsLnW1 = lnw, ldsLnT1 = lnt;
  const u32 hh = ln >> 5, t31 = ln & 31, q4 = l15 >> 2, xs = (t31 >> 1) & 7, lcA = (ln & 7) ^ (((ln >> 4) & 1) << 2);
  const u32 tb = (8 * hh + q4) * 128 + (((4 * cg + 2 * ((ln >> 4) & 1) + ((ln & 3) >> 1)) ^ ((q4 >> 1) << 2)) << 4) + (ln & 1) * 8;
  const u32 ldsT0 = OPQ(tb), ldsT1 = OPQ(tb + 49152), ldsT2 = OPQ(tb + 98304);
  const u32 ldsXf0 = t31 * 1152 + (((0 + hh) ^ xs) << 4), ldsXf1 = t31 * 1152 + (((2 + hh) ^ xs) << 4), ldsXf2 = t31 * 1152 + (((4 + hh) ^ xs) << 4), ldsXf3 = t31 * 1152 + (((6 + hh) ^ xs) << 4);
  const u32 voffH = (2 * rg + (t31 >> 4)) * 1024 + hh * 256 + l15 * 16;
  const u32 lW = w * 3072;
  const u32 sPF = WS_PFLAG + ((r * 4 + j) * 4 + c) * 1024 + w * 128;
  u32 pfv = 0;
  if (w == 7) {
    u64 pc; u32 len;
    asm volatile("s_getpc_b64 %0\n\tL_code_begin_%=:\n\ts_mov_b32 %1, L_code_end-L_code_begin_%=" : "=s"(pc), "=s"(len));
    const u32x4 srdC = mk_srd((const void*)(pc & ~127ull), len & ~127u);
    const u32 vo = ((idx & 15) * 64 + ln) * 128;
    asm volatile("buffer_load_dword %0, %1, %2, 0 offen" : "=v"(pfv) : "v"(vo), "s"(srdC) : "memory");
  }
  SB;
  SB; asm volatile("s_bitcmp1_b32 255, %[cr]\n\ts_cbranch_scc0 1f\n\ts_waitcnt vmcnt(0)\n1:\n\ts_barrier" : "+v"(fx) : [cr] "s"(w) : "memory", "scc"); SB;
  LOAD4(xr[0][0], voffX, srdX, sX + 0 * 36864, 0, "");
  LOAD4(xr[0][1], voffX, srdX, sX + 0 * 36864, 256, "");
  LOAD4(xr[0][2], voffX, srdX, sX + 0 * 36864, 512, "");
  LOAD4(xr[0][3], voffX, srdX, sX + 0 * 36864, 768, "");
  LOAD2(xt[0], voffXt, srdX, sX + 0 * 36864, 1024, "");
  asm volatile("s_barrier" ::: "memory");
  LOAD4(xr[1][0], voffX, srdX, sX + 1 * 36864, 0, "");
  LOAD4(xr[1][1], voffX, srdX, sX + 1 * 36864, 256, "");
  LOAD4(xr[1][2], voffX, srdX, sX + 1 * 36864, 512, "");
  LOAD4(xr[1][3], voffX, srdX, sX + 1 * 36864, 768, "");
  LOAD2(xt[1], voffXt, srdX, sX + 1 * 36864, 1024, "");
  LOAD4(xr[2][0], voffX, srdX, sX + 2 * 36864, 0, "");
  LOAD4(xr[2][1], voffX, srdX, sX + 2 * 36864, 256, "");
  LOAD4(xr[2][2], voffX, srdX, sX + 2 * 36864, 512, "");
  LOAD4(xr[2][3], voffX, srdX, sX + 2 * 36864, 768, "");
  LOAD2(xt[2], voffXt, srdX, sX + 2 * 36864, 1024, "");
  LOAD2_C(w, eq, 7, ep, pfo(ln, 1), srdWS, 0, 0, " sc1");
  LOAD1_C(w, lt, 3, bia, voffB, srdB1, sB1);
  LOAD1K_C(w, eq, 6, bia, vzero + ln * 4, srdB2, (192 * c + (j & 1) * 96) * 2);
  u32 pf0 = 0, pf2 = 0; const u32 pw8 = (bid - 64) * 8 + w;
  SB; asm volatile("s_bitcmp1_b32 56, %[cr]\n\ts_cbranch_scc0 1f\n\ts_waitcnt vmcnt(10)\n1:\n\ts_bitcmp1_b32 199, %[cr]\n\ts_cbranch_scc0 2f\n\ts_waitcnt vmcnt(11)\n2:\n\t" : "+v"(xr[0][0]), "+v"(xr[0][1]), "+v"(xr[0][2]), "+v"(xr[0][3]), "+v"(xt[0]) : [cr] "s"(w) : "memory", "scc"); SB;
  LOAD4(xr[3][0], voffX, srdX, sX + 3 * 36864, 0, "");
  LOAD4(xr[3][1], voffX, srdX, sX + 3 * 36864, 256, "");
  LOAD4(xr[3][2], voffX, srdX, sX + 3 * 36864, 512, "");
  LOAD4(xr[3][3], voffX, srdX, sX + 3 * 36864, 768, "");
  LOAD2(xt[3], voffXt, srdX, sX + 3 * 36864, 1024, "");
  LOAD1K_C(wp, eq, 2, ft, ln >= 36 ? OOB : ((r & 15) * 36 + (w & 1) * 18 + ln % 18) * 9216 + (ln / 18) * 128, srdW, sB1);
  float rl0, lc0; ln_stats(xr[0], xt[0], rl0, lc0); SB;
  ln_apply<0, 5>(xr[0], xt[0], rl0, lc0, ldsLnW0, ldsLnT0); SB;
  DMA2(lW, 49152, voffWa, srdW, sW, 0);
  DMA2(lW, 50176, voffWb, srdW, sW, 73728);
  DMA2(lW, 51200, voffWa, srdW, sW, 147456);
  SB; asm volatile("s_bitcmp1_b32 8, %[cr]\n\ts_cbranch_scc0 1f\n\ts_waitcnt vmcnt(13)\n1:\n\ts_bitcmp1_b32 247, %[cr]\n\ts_cbranch_scc0 2f\n\ts_waitcnt vmcnt(14)\n2:\n\t" : "+v"(xr[1][0]), "+v"(xr[1][1]), "+v"(xr[1][2]), "+v"(xr[1][3]), "+v"(xt[1]) : [cr] "s"(w) : "memory", "scc"); SB;
  float rl1, lc1; ln_stats(xr[1], xt[1], rl1, lc1); SB;
  SB; asm volatile("s_waitcnt lgkmcnt(0)\n\ts_barrier" :  : [cr] "s"(w) : "memory", "scc"); SB;
  if (rg == 0) {
    const u32 xa0 = OPQ(ldsXg0 + 122880), xa1 = OPQ(ldsXg1 + 122880);
    xf[0] = *(LAS s16x8*)(size_t)(xa0 + 0);
    xf[18] = *(LAS s16x8*)(size_t)(xa0 + 18432);
    xf[1] = *(LAS s16x8*)(size_t)(xa1 + 0);
    xf[19] = *(LAS s16x8*)(size_t)(xa1 + 18432);
    xf[2] = *(LAS s16x8*)(size_t)(xa0 + 128);
    xf[20] = *(LAS s16x8*)(size_t)(xa0 + 18560);
    xf[3] = *(LAS s16x8*)(size_t)(xa1 + 128);
    SB; ln_apply<0, 1>(xr[1], xt[1], rl1, lc1, ldsLnW1, ldsLnT1); SB;
    xf[21] = *(LAS s16x8*)(size_t)(xa1 + 18560);
    xf[4] = *(LAS s16x8*)(size_t)(xa0 + 256);
    xf[22] = *(LAS s16x8*)(size_t)(xa0 + 18688);
    xf[5] = *(LAS s16x8*)(size_t)(xa1 + 256);
    xf[23] = *(LAS s16x8*)(size_t)(xa1 + 18688);
    xf[6] = *(LAS s16x8*)(size_t)(xa0 + 384);
    xf[24] = *(LAS s16x8*)(size_t)(xa0 + 18816);
    SB; ln_apply<1, 2>(xr[1], xt[1], rl1, lc1, ldsLnW1, ldsLnT1); SB;
    xf[7] = *(LAS s16x8*)(size_t)(xa1 + 384);
    xf[25] = *(LAS s16x8*)(size_t)(xa1 + 18816);
    xf[8] = *(LAS s16x8*)(size_t)(xa0 + 512);
    xf[26] = *(LAS s16x8*)(size_t)(xa0 + 18944);
    xf[9] = *(LAS s16x8*)(size_t)(xa1 + 512);
    xf[27] = *(LAS s16x8*)(size_t)(xa1 + 18944);
    xf[10] = *(LAS s16x8*)(size_t)(xa0 + 640);
    SB; ln_apply<2, 3>(xr[1], xt[1], rl1, lc1, ldsLnW1, ldsLnT1); SB;
    xf[28] = *(LAS s16x8*)(size_t)(xa0 + 19072);
    xf[11] = *(LAS s16x8*)(size_t)(xa1 + 640);
    xf[29] = *(LAS s16x8*)(size_t)(xa1 + 19072);
    xf[12] = *(LAS s16x8*)(size_t)(xa0 + 768);
    xf[30] = *(LAS s16x8*)(size_t)(xa0 + 19200);
    xf[13] = *(LAS s16x8*)(size_t)(xa1 + 768);
    xf[31] = *(LAS s16x8*)(size_t)(xa1 + 19200);
    SB; ln_apply<3, 4>(xr[1], xt[1], rl1, lc1, ldsLnW1, ldsLnT1); SB;
    xf[14] = *(LAS s16x8*)(size_t)(xa0 + 896);
    xf[32] = *(LAS s16x8*)(size_t)(xa0 + 19328);
    xf[15] = *(LAS s16x8*)(size_t)(xa1 + 896);
    xf[33] = *(LAS s16x8*)(size_t)(xa1 + 19328);
    xf[16] = *(LAS s16x8*)(size_t)(xa0 + 1024);
    xf[34] = *(LAS s16x8*)(size_t)(xa0 + 19456);
    xf[17] = *(LAS s16x8*)(size_t)(xa1 + 1024);
    xf[35] = *(LAS s16x8*)(size_t)(xa1 + 19456);
    SB; ln_apply<4, 5>(xr[1], xt[1], rl1, lc1, ldsLnW1, ldsLnT1); SB;
  } else { ln_apply<0, 5>(xr[1], xt[1], rl1, lc1, ldsLnW1, ldsLnT1); SB; }
  DMA2(lW, 73728, voffWa, srdW, sW, 1769472);
  DMA2(lW, 74752, voffWb, srdW, sW, 1843200);
  DMA2(lW, 75776, voffWa, srdW, sW, 1916928);
  LOAD1K(pf0, pfo(ln, 27), srdW, pw8 * 27 * 128);
  SB; asm volatile("s_bitcmp1_b32 48, %[cr]\n\ts_cbranch_scc0 1f\n\ts_waitcnt vmcnt(7)\n1:\n\ts_bitcmp1_b32 199, %[cr]\n\ts_cbranch_scc0 2f\n\ts_waitcnt vmcnt(12)\n2:\n\t" : "+v"(ep), "+v"(pfv), "+v"(ft), "+v"(bia) : [cr] "s"(w) : "memory", "scc"); SB;
  if (tid < 192) { f32x2 bv; bv[0] = blo(bia); bv[1] = bhi(bia); *(LAS f32x2*)(size_t)(159744 + tid * 8) = bv; }
  if (w == 6 && ln < 48) *(LAS u32*)(size_t)(161280 + ln * 4) = bia;
  if (tid == 448) *(LAS u32x2*)(size_t)162816 = ep;
  SB; asm volatile("s_bitcmp1_b32 8, %[cr]\n\ts_cbranch_scc0 1f\n\ts_waitcnt vmcnt(12)\n1:\n\t" : "+v"(xr[2][0]), "+v"(xr[2][1]), "+v"(xr[2][2]), "+v"(xr[2][3]), "+v"(xt[2]) : [cr] "s"(w) : "memory", "scc"); SB;
  float rl2, lc2; ln_stats(xr[2], xt[2], rl2, lc2); SB;
  SB; asm volatile("s_waitcnt lgkmcnt(0)\n\ts_barrier" :  : [cr] "s"(w) : "memory", "scc"); SB;
  if (rg == 1) {
    const u32 xa0 = OPQ(ldsXg0 + 0), xa1 = OPQ(ldsXg1 + 0);
    xf[0] = *(LAS s16x8*)(size_t)(xa0 + 0);
    xf[18] = *(LAS s16x8*)(size_t)(xa0 + 18432);
    xf[1] = *(LAS s16x8*)(size_t)(xa1 + 0);
    xf[19] = *(LAS s16x8*)(size_t)(xa1 + 18432);
    xf[2] = *(LAS s16x8*)(size_t)(xa0 + 128);
    xf[20] = *(LAS s16x8*)(size_t)(xa0 + 18560);
    xf[3] = *(LAS s16x8*)(size_t)(xa1 + 128);
    SB; ln_apply<0, 1>(xr[2], xt[2], rl2, lc2, ldsLnW0, ldsLnT0); SB;
    xf[21] = *(LAS s16x8*)(size_t)(xa1 + 18560);
    xf[4] = *(LAS s16x8*)(size_t)(xa0 + 256);
    xf[22] = *(LAS s16x8*)(size_t)(xa0 + 18688);
    xf[5] = *(LAS s16x8*)(size_t)(xa1 + 256);
    xf[23] = *(LAS s16x8*)(size_t)(xa1 + 18688);
    xf[6] = *(LAS s16x8*)(size_t)(xa0 + 384);
    xf[24] = *(LAS s16x8*)(size_t)(xa0 + 18816);
    SB; ln_apply<1, 2>(xr[2], xt[2], rl2, lc2, ldsLnW0, ldsLnT0); SB;
    xf[7] = *(LAS s16x8*)(size_t)(xa1 + 384);
    xf[25] = *(LAS s16x8*)(size_t)(xa1 + 18816);
    xf[8] = *(LAS s16x8*)(size_t)(xa0 + 512);
    xf[26] = *(LAS s16x8*)(size_t)(xa0 + 18944);
    xf[9] = *(LAS s16x8*)(size_t)(xa1 + 512);
    xf[27] = *(LAS s16x8*)(size_t)(xa1 + 18944);
    xf[10] = *(LAS s16x8*)(size_t)(xa0 + 640);
    SB; ln_apply<2, 3>(xr[2], xt[2], rl2, lc2, ldsLnW0, ldsLnT0); SB;
    xf[28] = *(LAS s16x8*)(size_t)(xa0 + 19072);
    xf[11] = *(LAS s16x8*)(size_t)(xa1 + 640);
    xf[29] = *(LAS s16x8*)(size_t)(xa1 + 19072);
    xf[12] = *(LAS s16x8*)(size_t)(xa0 + 768);
    xf[30] = *(LAS s16x8*)(size_t)(xa0 + 19200);
    xf[13] = *(LAS s16x8*)(size_t)(xa1 + 768);
    xf[31] = *(LAS s16x8*)(size_t)(xa1 + 19200);
    SB; ln_apply<3, 4>(xr[2], xt[2], rl2, lc2, ldsLnW0, ldsLnT0); SB;
    xf[14] = *(LAS s16x8*)(size_t)(xa0 + 896);
    xf[32] = *(LAS s16x8*)(size_t)(xa0 + 19328);
    xf[15] = *(LAS s16x8*)(size_t)(xa1 + 896);
    xf[33] = *(LAS s16x8*)(size_t)(xa1 + 19328);
    xf[16] = *(LAS s16x8*)(size_t)(xa0 + 1024);
    xf[34] = *(LAS s16x8*)(size_t)(xa0 + 19456);
    xf[17] = *(LAS s16x8*)(size_t)(xa1 + 1024);
    xf[35] = *(LAS s16x8*)(size_t)(xa1 + 19456);
    SB; ln_apply<4, 5>(xr[2], xt[2], rl2, lc2, ldsLnW0, ldsLnT0); SB;
  } else { ln_apply<0, 5>(xr[2], xt[2], rl2, lc2, ldsLnW0, ldsLnT0); SB; }
  DMA2(lW, 98304, voffWa, srdW, sW, 3538944);
  DMA2(lW, 99328, voffWb, srdW, sW, 3612672);
  DMA2(lW, 100352, voffWa, srdW, sW, 3686400);
  LOAD1K(pf0, pfo(ln, 11), srdQ, pw8 * 11 * 128);
  SB; asm volatile("s_bitcmp1_b32 207, %[cr]\n\ts_cbranch_scc0 1f\n\ts_waitcnt vmcnt(11)\n1:\n\t" : "+v"(xr[3][0]), "+v"(xr[3][1]), "+v"(xr[3][2]), "+v"(xr[3][3]), "+v"(xt[3]) : [cr] "s"(w) : "memory", "scc"); SB;
  float rl3, lc3; ln_stats(xr[3], xt[3], rl3, lc3); SB;
  SB; asm volatile("s_waitcnt lgkmcnt(0)\n\ts_barrier" :  : [cr] "s"(w) : "memory", "scc"); SB;
  if (rg == 2) {
    const u32 xa0 = OPQ(ldsXg0 + 122880), xa1 = OPQ(ldsXg1 + 122880);
    xf[0] = *(LAS s16x8*)(size_t)(xa0 + 0);
    xf[18] = *(LAS s16x8*)(size_t)(xa0 + 18432);
    xf[1] = *(LAS s16x8*)(size_t)(xa1 + 0);
    xf[19] = *(LAS s16x8*)(size_t)(xa1 + 18432);
    xf[2] = *(LAS s16x8*)(size_t)(xa0 + 128);
    xf[20] = *(LAS s16x8*)(size_t)(xa0 + 18560);
    xf[3] = *(LAS s16x8*)(size_t)(xa1 + 128);
    SB; ln_apply<0, 1>(xr[3], xt[3], rl3, lc3, ldsLnW1, ldsLnT1); SB;
    xf[21] = *(LAS s16x8*)(size_t)(xa1 + 18560);
    xf[4] = *(LAS s16x8*)(size_t)(xa0 + 256);
    xf[22] = *(LAS s16x8*)(size_t)(xa0 + 18688);
    xf[5] = *(LAS s16x8*)(size_t)(xa1 + 256);
    xf[23] = *(LAS s16x8*)(size_t)(xa1 + 18688);
    xf[6] = *(LAS s16x8*)(size_t)(xa0 + 384);
    xf[24] = *(LAS s16x8*)(size_t)(xa0 + 18816);
    SB; ln_apply<1, 2>(xr[3], xt[3], rl3, lc3, ldsLnW1, ldsLnT1); SB;
    xf[7] = *(LAS s16x8*)(size_t)(xa1 + 384);
    xf[25] = *(LAS s16x8*)(size_t)(xa1 + 18816);
    xf[8] = *(LAS s16x8*)(size_t)(xa0 + 512);
    xf[26] = *(LAS s16x8*)(size_t)(xa0 + 18944);
    xf[9] = *(LAS s16x8*)(size_t)(xa1 + 512);
    xf[27] = *(LAS s16x8*)(size_t)(xa1 + 18944);
    xf[10] = *(LAS s16x8*)(size_t)(xa0 + 640);
    SB; ln_apply<2, 3>(xr[3], xt[3], rl3, lc3, ldsLnW1, ldsLnT1); SB;
    xf[28] = *(LAS s16x8*)(size_t)(xa0 + 19072);
    xf[11] = *(LAS s16x8*)(size_t)(xa1 + 640);
    xf[29] = *(LAS s16x8*)(size_t)(xa1 + 19072);
    xf[12] = *(LAS s16x8*)(size_t)(xa0 + 768);
    xf[30] = *(LAS s16x8*)(size_t)(xa0 + 19200);
    xf[13] = *(LAS s16x8*)(size_t)(xa1 + 768);
    xf[31] = *(LAS s16x8*)(size_t)(xa1 + 19200);
    SB; ln_apply<3, 4>(xr[3], xt[3], rl3, lc3, ldsLnW1, ldsLnT1); SB;
    xf[14] = *(LAS s16x8*)(size_t)(xa0 + 896);
    xf[32] = *(LAS s16x8*)(size_t)(xa0 + 19328);
    xf[15] = *(LAS s16x8*)(size_t)(xa1 + 896);
    xf[33] = *(LAS s16x8*)(size_t)(xa1 + 19328);
    xf[16] = *(LAS s16x8*)(size_t)(xa0 + 1024);
    xf[34] = *(LAS s16x8*)(size_t)(xa0 + 19456);
    xf[17] = *(LAS s16x8*)(size_t)(xa1 + 1024);
    xf[35] = *(LAS s16x8*)(size_t)(xa1 + 19456);
    SB; ln_apply<4, 5>(xr[3], xt[3], rl3, lc3, ldsLnW1, ldsLnT1); SB;
  } else { ln_apply<0, 5>(xr[3], xt[3], rl3, lc3, ldsLnW1, ldsLnT1); SB; }
  SB; asm volatile("s_bitcmp1_b32 255, %[cr]\n\ts_cbranch_scc0 1f\n\ts_waitcnt vmcnt(1)\n1:\n\ts_waitcnt lgkmcnt(0)\n\ts_barrier" :  : [cr] "s"(w) : "memory", "scc"); SB;
  if (rg == 3) {
    const u32 xa0 = OPQ(ldsXg0 + 0), xa1 = OPQ(ldsXg1 + 0);
    xf[0] = *(LAS s16x8*)(size_t)(xa0 + 0);
    xf[18] = *(LAS s16x8*)(size_t)(xa0 + 18432);
    xf[1] = *(LAS s16x8*)(size_t)(xa1 + 0);
    xf[19] = *(LAS s16x8*)(size_t)(xa1 + 18432);
    xf[2] = *(LAS s16x8*)(size_t)(xa0 + 128);
    xf[20] = *(LAS s16x8*)(size_t)(xa0 + 18560);
    xf[3] = *(LAS s16x8*)(size_t)(xa1 + 128);
    xf[21] = *(LAS s16x8*)(size_t)(xa1 + 18560);
    xf[4] = *(LAS s16x8*)(size_t)(xa0 + 256);
    xf[22] = *(LAS s16x8*)(size_t)(xa0 + 18688);
    xf[5] = *(LAS s16x8*)(size_t)(xa1 + 256);
    xf[23] = *(LAS s16x8*)(size_t)(xa1 + 18688);
    xf[6] = *(LAS s16x8*)(size_t)(xa0 + 384);
    xf[24] = *(LAS s16x8*)(size_t)(xa0 + 18816);
    xf[7] = *(LAS s16x8*)(size_t)(xa1 + 384);
    xf[25] = *(LAS s16x8*)(size_t)(xa1 + 18816);
    xf[8] = *(LAS s16x8*)(size_t)(xa0 + 512);
    xf[26] = *(LAS s16x8*)(size_t)(xa0 + 18944);
    xf[9] = *(LAS s16x8*)(size_t)(xa1 + 512);
    xf[27] = *(LAS s16x8*)(size_t)(xa1 + 18944);
    xf[10] = *(LAS s16x8*)(size_t)(xa0 + 640);
    xf[28] = *(LAS s16x8*)(size_t)(xa0 + 19072);
    xf[11] = *(LAS s16x8*)(size_t)(xa1 + 640);
    xf[29] = *(LAS s16x8*)(size_t)(xa1 + 19072);
    xf[12] = *(LAS s16x8*)(size_t)(xa0 + 768);
    xf[30] = *(LAS s16x8*)(size_t)(xa0 + 19200);
    xf[13] = *(LAS s16x8*)(size_t)(xa1 + 768);
    xf[31] = *(LAS s16x8*)(size_t)(xa1 + 19200);
    xf[14] = *(LAS s16x8*)(size_t)(xa0 + 896);
    xf[32] = *(LAS s16x8*)(size_t)(xa0 + 19328);
    xf[15] = *(LAS s16x8*)(size_t)(xa1 + 896);
    xf[33] = *(LAS s16x8*)(size_t)(xa1 + 19328);
    xf[16] = *(LAS s16x8*)(size_t)(xa0 + 1024);
    xf[34] = *(LAS s16x8*)(size_t)(xa0 + 19456);
    xf[17] = *(LAS s16x8*)(size_t)(xa1 + 1024);
    xf[35] = *(LAS s16x8*)(size_t)(xa1 + 19456);
  }
  ep = *(LAS u32x2*)(size_t)162816;
  const u64 E0 = ((u64)(u32)__builtin_amdgcn_readfirstlane(ep[1]) << 32) | (u32)__builtin_amdgcn_readfirstlane(ep[0]);
  const u64 TAG = (E0 * 6364136223846793005ull + 0x9E3779B97F4A7C1Dull) & ~7ull;
  const u32 tagLo = (u32)TAG, tagHi = (u32)(TAG >> 32);
  u32 tch = 0; f32x4 A0_0, A0_1, A0_2, A0_3, A1_0, A1_1, A1_2, A1_3; f32x16 A0, A1; u32x4 hp0, hp1; const u32 vl16 = ln * 16, th = j >> 1, fin = (rg >> 1) ^ th;
  const u32 voffHP = ln >= 2 ? OOB : 4096 + ((r * 4 + j) * 4 + (c + 1 + ln) % 3) * 1024 + w * 128;
  A0_0 = *(volatile LAS f32x4*)(size_t)(ldsBias + 0);
  A0_1 = *(volatile LAS f32x4*)(size_t)(ldsBias + 0);
  A0_2 = *(volatile LAS f32x4*)(size_t)(ldsBias + 64);
  A0_3 = *(volatile LAS f32x4*)(size_t)(ldsBias + 64);
  s16x8 w0 = TR2(ldsA1, 0, 512);
  s16x8 w1 = TR2(ldsB1, 0, 512);
  s16x8 w2 = TR2(ldsA1, 4096, 4608);
  AM16N(A0_0, w0, xf[0], w1); SB;
  s16x8 w3 = TR2(ldsB1, 4096, 4608);
  SB; AM16(A0_1, w0, xf[18]);
  SB;
  AM16(A0_2, w1, xf[0]); SB;
  s16x8 w4 = TR2(ldsA1, 8192, 8704);
  SB; AM16(A0_3, w1, xf[18]);
  SB;
  AM16N(A0_0, w2, xf[1], w3); SB;
  s16x8 w5 = TR2(ldsB1, 8192, 8704);
  SB; AM16(A0_1, w2, xf[19]);
  DMA2(lW, 122880, voffWa, srdW, sW, 128);
  SB;
  AM16(A0_2, w3, xf[1]); SB;
  s16x8 w6 = TR2(ldsA1, 12288, 12800);
  SB; AM16(A0_3, w3, xf[19]);
  A1_0 = *(volatile LAS f32x4*)(size_t)(ldsBias + 256);
  SB;
  AM16N(A0_0, w4, xf[2], w5); SB;
  s16x8 w7 = TR2(ldsB1, 12288, 12800);
  SB; AM16(A0_1, w4, xf[20]);
  SB;
  AM16(A0_2, w5, xf[2]); SB;
  s16x8 w8 = TR2(ldsA1, 16384, 16896);
  SB; AM16(A0_3, w5, xf[20]);
  A1_1 = *(volatile LAS f32x4*)(size_t)(ldsBias + 256);
  SB;
  AM16N(A0_0, w6, xf[3], w7); SB;
  s16x8 w9 = TR2(ldsB1, 16384, 16896);
  SB; AM16(A0_1, w6, xf[21]);
  DMA2(lW, 123904, voffWb, srdW, sW, 73856);
  SB;
  AM16(A0_2, w7, xf[3]); SB;
  s16x8 w10 = TR2(ldsA1, 20480, 20992);
  SB; AM16(A0_3, w7, xf[21]);
  A1_2 = *(volatile LAS f32x4*)(size_t)(ldsBias + 320);
  SB;
  AM16N(A0_0, w8, xf[4], w9); SB;
  s16x8 w11 = TR2(ldsB1, 20480, 20992);
  SB; AM16(A0_1, w8, xf[22]);
  SB;
  AM16(A0_2, w9, xf[4]); SB;
  s16x8 w12 = TR2(ldsA1, 24576, 25088);
  SB; AM16(A0_3, w9, xf[22]);
  A1_3 = *(volatile LAS f32x4*)(size_t)(ldsBias + 320);
  SB;
  AM16N(A0_0, w10, xf[5], w11); SB;
  s16x8 w13 = TR2(ldsB1, 24576, 25088);
  SB; AM16(A0_1, w10, xf[23]);
  DMA2(lW, 124928, voffWa, srdW, sW, 147584);
  SB;
  AM16(A0_2, w11, xf[5]); SB;
  s16x8 w14 = TR2(ldsA1, 28672, 29184);
  SB; AM16(A0_3, w11, xf[23]);
  SB;
  SB; asm volatile("s_waitcnt lgkmcnt(0)\n\ts_barrier" :  :: "memory"); SB;
  AM16N(A0_0, w12, xf[6], w13); SB;
  s16x8 w15 = TR2(ldsB1, 28672, 29184);
  SB; AM16(A0_1, w12, xf[24]);
  u32 voffV0; { const u32 bb = w * 3072 + 0 + vl16; const u32 rw = bb / 384, cb = bb - rw * 384; voffV0 = OPQ(rw * 1152 + (cb ^ (((rw >> 1) & 3) << 5))); }
  SB;
  AM16(A0_2, w13, xf[6]); SB;
  s16x8 w16 = TR2(ldsA1, 32768, 33280);
  SB; AM16(A0_3, w13, xf[24]);
  DMA2(lW, 0, voffWa, srdW, sW, 1769600);
  SB;
  AM16N(A0_0, w14, xf[7], w15); SB;
  s16x8 w17 = TR2(ldsB1, 32768, 33280);
  SB; AM16(A0_1, w14, xf[25]);
  DMA2(lW, 1024, voffWb, srdW, sW, 1843328);
  SB;
  AM16(A0_2, w15, xf[7]); SB;
  s16x8 w18 = TR2(ldsA1, 36864, 37376);
  SB; AM16(A0_3, w15, xf[25]);
  SB;
  AM16N(A0_0, w16, xf[8], w17); SB;
  s16x8 w19 = TR2(ldsB1, 36864, 37376);
  SB; AM16(A0_1, w16, xf[26]);
  DMA2(lW, 2048, voffWa, srdW, sW, 1917056);
  SB;
  AM16(A0_2, w17, xf[8]); SB;
  s16x8 w20 = TR2(ldsA1, 40960, 41472);
  SB; AM16(A0_3, w17, xf[26]);
  DMA2(lW, 24576, voffWa, srdW, sW, 3539072);
  SB;
  AM16N(A0_0, w18, xf[9], w19); SB;
  s16x8 w21 = TR2(ldsB1, 40960, 41472);
  SB; AM16(A0_1, w18, xf[27]);
  DMA2(lW, 25600, voffWb, srdW, sW, 3612800);
  SB;
  AM16(A0_2, w19, xf[9]); SB;
  s16x8 w22 = TR2(ldsA1, 45056, 45568);
  SB; AM16(A0_3, w19, xf[27]);
  DMA2(lW, 26624, voffWa, srdW, sW, 3686528);
  SB;
  AM16N(A0_0, w20, xf[10], w21); SB;
  s16x8 w23 = TR2(ldsB1, 45056, 45568);
  SB; AM16(A0_1, w20, xf[28]);
  DMA2(lW, 49152, voffWa, srdW, sW, 256);
  SB;
  AM16(A0_2, w21, xf[10]); SB;
  s16x8 w24 = TR2(ldsA2, 0, 512);
  SB; AM16(A0_3, w21, xf[28]);
  u32 voffV1; { const u32 bb = w * 3072 + 1024 + vl16; const u32 rw = bb / 384, cb = bb - rw * 384; voffV1 = OPQ(rw * 1152 + (cb ^ (((rw >> 1) & 3) << 5))); }
  SB;
  AM16N(A0_0, w22, xf[11], w23); SB;
  s16x8 w25 = TR2(ldsB2, 0, 512);
  SB; AM16(A0_1, w22, xf[29]);
  DMA2(lW, 50176, voffWb, srdW, sW, 73984);
  SB;
  AM16(A0_2, w23, xf[11]); SB;
  s16x8 w26 = TR2(ldsA2, 4096, 4608);
  SB; AM16(A0_3, w23, xf[29]);
  DMA2(lW, 51200, voffWa, srdW, sW, 147712);
  SB;
  SB; asm volatile("s_waitcnt vmcnt(9)\n\ts_barrier" :  :: "memory"); SB;
  AM16N(A0_0, w24, xf[12], w25); SB;
  s16x8 w27 = TR2(ldsB2, 4096, 4608);
  SB; AM16(A0_1, w24, xf[30]);
  SB;
  AM16(A0_2, w25, xf[12]); SB;
  s16x8 w28 = TR2(ldsA2, 8192, 8704);
  SB; AM16(A0_3, w25, xf[30]);
  SB;
  AM16N(A0_0, w26, xf[13], w27); SB;
  s16x8 w29 = TR2(ldsB2, 8192, 8704);
  SB; AM16(A0_1, w26, xf[31]);
  DMA2(lW, 73728, voffWa, srdW, sW, 1769728);
  SB;
  AM16(A0_2, w27, xf[13]); SB;
  s16x8 w30 = TR2(ldsA2, 12288, 12800);
  SB; AM16(A0_3, w27, xf[31]);
  SB;
  AM16N(A0_0, w28, xf[14], w29); SB;
  s16x8 w31 = TR2(ldsB2, 12288, 12800);
  SB; AM16(A0_1, w28, xf[32]);
  SB;
  AM16(A0_2, w29, xf[14]); SB;
  s16x8 w32 = TR2(ldsA2, 16384, 16896);
  SB; AM16(A0_3, w29, xf[32]);
  SB;
  AM16N(A0_0, w30, xf[15], w31); SB;
  s16x8 w33 = TR2(ldsB2, 16384, 16896);
  SB; AM16(A0_1, w30, xf[33]);
  u32 voffV2; { const u32 bb = w * 3072 + 2048 + vl16; const u32 rw = bb / 384, cb = bb - rw * 384; voffV2 = OPQ(rw * 1152 + (cb ^ (((rw >> 1) & 3) << 5))); }
  DMA2(lW, 74752, voffWb, srdW, sW, 1843456);
  SB;
  AM16(A0_2, w31, xf[15]); SB;
  s16x8 w34 = TR2(ldsA2, 20480, 20992);
  SB; AM16(A0_3, w31, xf[33]);
  SB;
  AM16N(A0_0, w32, xf[16], w33); SB;
  s16x8 w35 = TR2(ldsB2, 20480, 20992);
  SB; AM16(A0_1, w32, xf[34]);
  SB;
  AM16(A0_2, w33, xf[16]); SB;
  s16x8 w36 = TR2(ldsA2, 24576, 25088);
  SB; AM16(A0_3, w33, xf[34]);
  SB;
  AM16N(A0_0, w34, xf[17], w35); SB;
  s16x8 w37 = TR2(ldsB2, 24576, 25088);
  SB; AM16(A0_1, w34, xf[35]);
  const u32 sV = (1152 * j) * 1152 + 384 * c;
  DMA2(lW, 75776, voffWa, srdW, sW, 1917184);
  SB;
  AM16(A0_2, w35, xf[17]); SB;
  s16x8 w38 = TR2(ldsA2, 28672, 29184);
  SB; AM16(A0_3, w35, xf[35]);
  SB;
  SB; asm volatile("s_waitcnt vmcnt(9)\n\ts_barrier" :  :: "memory"); SB;
  AM16N(A1_0, w36, xf[0], w37); SB;
  s16x8 w39 = TR2(ldsB2, 28672, 29184);
  SB; AM16(A1_1, w36, xf[18]);
  SB;
  AM16(A1_2, w37, xf[0]); SB;
  s16x8 w40 = TR2(ldsA2, 32768, 33280);
  float g0_0a = A0_0[0] * A0_0[0];
  float g0_0b = A0_0[1] * A0_0[1];
  SB; AM16(A1_3, w37, xf[18]);
  g0_0a = __builtin_fmaf(g0_0a, GB, GA);
  g0_0b = __builtin_fmaf(g0_0b, GB, GA);
  g0_0a = g0_0a * A0_0[0];
  SB;
  AM16N(A1_0, w38, xf[1], w39); SB;
  s16x8 w41 = TR2(ldsB2, 32768, 33280);
  g0_0b = g0_0b * A0_0[1];
  SB; AM16(A1_1, w38, xf[19]);
  g0_0a = __builtin_amdgcn_exp2f(g0_0a);
  g0_0b = __builtin_amdgcn_exp2f(g0_0b);
  DMA2(lW, 98304, voffWa, srdW, sW, 3539200);
  SB;
  AM16(A1_2, w39, xf[1]); SB;
  s16x8 w42 = TR2(ldsA2, 36864, 37376);
  g0_0a = g0_0a + 1.0f;
  SB; AM16(A1_3, w39, xf[19]);
  g0_0b = g0_0b + 1.0f;
  g0_0a = __builtin_amdgcn_rcpf(g0_0a);
  SB;
  AM16N(A1_0, w40, xf[2], w41); SB;
  s16x8 w43 = TR2(ldsB2, 36864, 37376);
  g0_0b = __builtin_amdgcn_rcpf(g0_0b);
  SB; AM16(A1_1, w40, xf[20]);
  g0_0a = g0_0a * A0_0[0];
  g0_0b = g0_0b * A0_0[1];
  SB;
  AM16(A1_2, w41, xf[2]); SB;
  s16x8 w44 = TR2(ldsA2, 40960, 41472);
  hp0[0] = cvtpk(g0_0a, g0_0b);
  float g0_1a = A0_0[2] * A0_0[2];
  SB; AM16(A1_3, w41, xf[20]);
  float g0_1b = A0_0[3] * A0_0[3];
  g0_1a = __builtin_fmaf(g0_1a, GB, GA);
  g0_1b = __builtin_fmaf(g0_1b, GB, GA);
  SB;
  AM16N(A1_0, w42, xf[3], w43); SB;
  s16x8 w45 = TR2(ldsB2, 40960, 41472);
  g0_1a = g0_1a * A0_0[2];
  SB; AM16(A1_1, w42, xf[21]);
  g0_1b = g0_1b * A0_0[3];
  g0_1a = __builtin_amdgcn_exp2f(g0_1a);
  DMA2(lW, 99328, voffWb, srdW, sW, 3612928);
  SB;
  AM16(A1_2, w43, xf[3]); SB;
  s16x8 w46 = TR2(ldsA2, 45056, 45568);
  g0_1b = __builtin_amdgcn_exp2f(g0_1b);
  SB; AM16(A1_3, w43, xf[21]);
  g0_1a = g0_1a + 1.0f;
  g0_1b = g0_1b + 1.0f;
  SB;
  AM16N(A1_0, w44, xf[4], w45); SB;
  s16x8 w47 = TR2(ldsB2, 45056, 45568);
  g0_1a = __builtin_amdgcn_rcpf(g0_1a);
  SB; AM16(A1_1, w44, xf[22]);
  g0_1b = __builtin_amdgcn_rcpf(g0_1b);
  g0_1a = g0_1a * A0_0[2];
  SB;
  AM16(A1_2, w45, xf[4]); SB;
  s16x8 w48 = TR2(ldsA0, 0, 512);
  g0_1b = g0_1b * A0_0[3];
  hp0[1] = cvtpk(g0_1a, g0_1b);
  A0_0 = *(volatile LAS f32x4*)(size_t)(ldsBias + 512);
  SB; AM16(A1_3, w45, xf[22]);
  float g0_2a = A0_2[0] * A0_2[0];
  float g0_2b = A0_2[1] * A0_2[1];
  g0_2a = __builtin_fmaf(g0_2a, GB, GA);
  SB;
  AM16N(A1_0, w46, xf[5], w47); SB;
  s16x8 w49 = TR2(ldsB0, 0, 512);
  g0_2b = __builtin_fmaf(g0_2b, GB, GA);
  g0_2a = g0_2a * A0_2[0];
  SB; AM16(A1_1, w46, xf[23]);
  g0_2b = g0_2b * A0_2[1];
  g0_2a = __builtin_amdgcn_exp2f(g0_2a);
  DMA2(lW, 100352, voffWa, srdW, sW, 3686656);
  SB;
  AM16(A1_2, w47, xf[5]); SB;
  s16x8 w50 = TR2(ldsA0, 4096, 4608);
  g0_2b = __builtin_amdgcn_exp2f(g0_2b);
  SB; AM16(A1_3, w47, xf[23]);
  g0_2a = g0_2a + 1.0f;
  g0_2b = g0_2b + 1.0f;
  SB;
  SB; asm volatile("s_waitcnt vmcnt(9)\n\ts_barrier" :  :: "memory"); SB;
  AM16N(A1_0, w48, xf[6], w49); SB;
  s16x8 w51 = TR2(ldsB0, 4096, 4608);
  g0_2a = __builtin_amdgcn_rcpf(g0_2a);
  SB; AM16(A1_1, w48, xf[24]);
  g0_2b = __builtin_amdgcn_rcpf(g0_2b);
  SB;
  AM16(A1_2, w49, xf[6]); SB;
  s16x8 w52 = TR2(ldsA0, 8192, 8704);
  g0_2a = g0_2a * A0_2[0];
  g0_2b = g0_2b * A0_2[1];
  SB; AM16(A1_3, w49, xf[24]);
  hp0[2] = cvtpk(g0_2a, g0_2b);
  float g0_3a = A0_2[2] * A0_2[2];
  float g0_3b = A0_2[3] * A0_2[3];
  SB;
  AM16N(A1_0, w50, xf[7], w51); SB;
  s16x8 w53 = TR2(ldsB0, 8192, 8704);
  g0_3a = __builtin_fmaf(g0_3a, GB, GA);
  g0_3b = __builtin_fmaf(g0_3b, GB, GA);
  SB; AM16(A1_1, w50, xf[25]);
  g0_3a = g0_3a * A0_2[2];
  g0_3b = g0_3b * A0_2[3];
  DMA2(lW, 122880, voffWa, srdW, sW, 384);
  SB;
  AM16(A1_2, w51, xf[7]); SB;
  s16x8 w54 = TR2(ldsA0, 12288, 12800);
  g0_3a = __builtin_amdgcn_exp2f(g0_3a);
  SB; AM16(A1_3, w51, xf[25]);
  g0_3b = __builtin_amdgcn_exp2f(g0_3b);
  g0_3a = g0_3a + 1.0f;
  SB;
  AM16N(A1_0, w52, xf[8], w53); SB;
  s16x8 w55 = TR2(ldsB0, 12288, 12800);
  g0_3b = g0_3b + 1.0f;
  SB; AM16(A1_1, w52, xf[26]);
  g0_3a = __builtin_amdgcn_rcpf(g0_3a);
  g0_3b = __builtin_amdgcn_rcpf(g0_3b);
  SB;
  AM16(A1_2, w53, xf[8]); SB;
  s16x8 w56 = TR2(ldsA0, 16384, 16896);
  g0_3a = g0_3a * A0_2[2];
  g0_3b = g0_3b * A0_2[3];
  SB; AM16(A1_3, w53, xf[26]);
  hp0[3] = cvtpk(g0_3a, g0_3b);
  A0_2 = *(volatile LAS f32x4*)(size_t)(ldsBias + 576);
  DMA2(lW, 123904, voffWb, srdW, sW, 74112);
  SB;
  AM16N(A1_0, w54, xf[9], w55); SB;
  s16x8 w57 = TR2(ldsB0, 16384, 16896);
  STORE4(hp0, vl16, srdWS, sHo + rg * 2048 + 0, 0);
  float g0_4a = A0_1[0] * A0_1[0];
  float g0_4b = A0_1[1] * A0_1[1];
  SB; AM16(A1_1, w54, xf[27]);
  g0_4a = __builtin_fmaf(g0_4a, GB, GA);
  g0_4b = __builtin_fmaf(g0_4b, GB, GA);
  g0_4a = g0_4a * A0_1[0];
  SB;
  AM16(A1_2, w55, xf[9]); SB;
  s16x8 w58 = TR2(ldsA0, 20480, 20992);
  g0_4b = g0_4b * A0_1[1];
  SB; AM16(A1_3, w55, xf[27]);
  g0_4a = __builtin_amdgcn_exp2f(g0_4a);
  g0_4b = __builtin_amdgcn_exp2f(g0_4b);
  DMA2(lW, 124928, voffWa, srdW, sW, 147840);
  SB;
  AM16N(A1_0, w56, xf[10], w57); SB;
  s16x8 w59 = TR2(ldsB0, 20480, 20992);
  g0_4a = g0_4a + 1.0f;
  SB; AM16(A1_1, w56, xf[28]);
  g0_4b = g0_4b + 1.0f;
  g0_4a = __builtin_amdgcn_rcpf(g0_4a);
  SB;
  AM16(A1_2, w57, xf[10]); SB;
  s16x8 w60 = TR2(ldsA0, 24576, 25088);
  g0_4b = __builtin_amdgcn_rcpf(g0_4b);
  SB; AM16(A1_3, w57, xf[28]);
  g0_4a = g0_4a * A0_1[0];
  g0_4b = g0_4b * A0_1[1];
  SB;
  AM16N(A1_0, w58, xf[11], w59); SB;
  s16x8 w61 = TR2(ldsB0, 24576, 25088);
  hp1[0] = cvtpk(g0_4a, g0_4b);
  float g0_5a = A0_1[2] * A0_1[2];
  SB; AM16(A1_1, w58, xf[29]);
  float g0_5b = A0_1[3] * A0_1[3];
  g0_5a = __builtin_fmaf(g0_5a, GB, GA);
  g0_5b = __builtin_fmaf(g0_5b, GB, GA);
  LOAD1K(pf2, pfo(ln, 27), srdW2, pw8 * 27 * 128);
  SB;
  AM16(A1_2, w59, xf[11]); SB;
  s16x8 w62 = TR2(ldsA0, 28672, 29184);
  g0_5a = g0_5a * A0_1[2];
  SB; AM16(A1_3, w59, xf[29]);
  g0_5b = g0_5b * A0_1[3];
  g0_5a = __builtin_amdgcn_exp2f(g0_5a);
  SB;
  SB; asm volatile("s_waitcnt vmcnt(11)\n\ts_barrier" :  :: "memory"); SB;
  AM16N(A1_0, w60, xf[12], w61); SB;
  s16x8 w63 = TR2(ldsB0, 28672, 29184);
  g0_5b = __builtin_amdgcn_exp2f(g0_5b);
  SB; AM16(A1_1, w60, xf[30]);
  g0_5a = g0_5a + 1.0f;
  g0_5b = g0_5b + 1.0f;
  SB;
  AM16(A1_2, w61, xf[12]); SB;
  s16x8 w64 = TR2(ldsA0, 32768, 33280);
  g0_5a = __builtin_amdgcn_rcpf(g0_5a);
  SB; AM16(A1_3, w61, xf[30]);
  g0_5b = __builtin_amdgcn_rcpf(g0_5b);
  g0_5a = g0_5a * A0_1[2];
  SB;
  AM16N(A1_0, w62, xf[13], w63); SB;
  s16x8 w65 = TR2(ldsB0, 32768, 33280);
  g0_5b = g0_5b * A0_1[3];
  hp1[1] = cvtpk(g0_5a, g0_5b);
  A0_1 = *(volatile LAS f32x4*)(size_t)(ldsBias + 512);
  SB; AM16(A1_1, w62, xf[31]);
  float g0_6a = A0_3[0] * A0_3[0];
  float g0_6b = A0_3[1] * A0_3[1];
  g0_6a = __builtin_fmaf(g0_6a, GB, GA);
  DMA2(lW, 0, voffWa, srdW, sW, 1769856);
  SB;
  AM16(A1_2, w63, xf[13]); SB;
  s16x8 w66 = TR2(ldsA0, 36864, 37376);
  g0_6b = __builtin_fmaf(g0_6b, GB, GA);
  g0_6a = g0_6a * A0_3[0];
  SB; AM16(A1_3, w63, xf[31]);
  g0_6b = g0_6b * A0_3[1];
  g0_6a = __builtin_amdgcn_exp2f(g0_6a);
  SB;
  AM16N(A1_0, w64, xf[14], w65); SB;
  s16x8 w67 = TR2(ldsB0, 36864, 37376);
  g0_6b = __builtin_amdgcn_exp2f(g0_6b);
  SB; AM16(A1_1, w64, xf[32]);
  g0_6a = g0_6a + 1.0f;
  g0_6b = g0_6b + 1.0f;
  SB;
  AM16(A1_2, w65, xf[14]); SB;
  s16x8 w68 = TR2(ldsA0, 40960, 41472);
  g0_6a = __builtin_amdgcn_rcpf(g0_6a);
  SB; AM16(A1_3, w65, xf[32]);
  g0_6b = __builtin_amdgcn_rcpf(g0_6b);
  SB;
  AM16N(A1_0, w66, xf[15], w67); SB;
  s16x8 w69 = TR2(ldsB0, 40960, 41472);
  g0_6a = g0_6a * A0_3[0];
  g0_6b = g0_6b * A0_3[1];
  SB; AM16(A1_1, w66, xf[33]);
  hp1[2] = cvtpk(g0_6a, g0_6b);
  float g0_7a = A0_3[2] * A0_3[2];
  float g0_7b = A0_3[3] * A0_3[3];
  DMA2(lW, 1024, voffWb, srdW, sW, 1843584);
  SB;
  AM16(A1_2, w67, xf[15]); SB;
  s16x8 w70 = TR2(ldsA0, 45056, 45568);
  g0_7a = __builtin_fmaf(g0_7a, GB, GA);
  g0_7b = __builtin_fmaf(g0_7b, GB, GA);
  SB; AM16(A1_3, w67, xf[33]);
  g0_7a = g0_7a * A0_3[2];
  g0_7b = g0_7b * A0_3[3];
  SB;
  AM16N(A1_0, w68, xf[16], w69); SB;
  s16x8 w71 = TR2(ldsB0, 45056, 45568);
  g0_7a = __builtin_amdgcn_exp2f(g0_7a);
  SB; AM16(A1_1, w68, xf[34]);
  g0_7b = __builtin_amdgcn_exp2f(g0_7b);
  g0_7a = g0_7a + 1.0f;
  SB;
  AM16(A1_2, w69, xf[16]); SB;
  s16x8 w72 = TR2(ldsA1, 0, 512);
  g0_7b = g0_7b + 1.0f;
  SB; AM16(A1_3, w69, xf[34]);
  g0_7a = __builtin_amdgcn_rcpf(g0_7a);
  g0_7b = __builtin_amdgcn_rcpf(g0_7b);
  SB;
  AM16N(A1_0, w70, xf[17], w71); SB;
  s16x8 w73 = TR2(ldsB1, 0, 512);
  g0_7a = g0_7a * A0_3[2];
  g0_7b = g0_7b * A0_3[3];
  SB; AM16(A1_1, w70, xf[35]);
  hp1[3] = cvtpk(g0_7a, g0_7b);
  A0_3 = *(volatile LAS f32x4*)(size_t)(ldsBias + 576);
  DMA2(lW, 2048, voffWa, srdW, sW, 1917312);
  SB;
  AM16(A1_2, w71, xf[17]); SB;
  s16x8 w74 = TR2(ldsA1, 4096, 4608);
  SB; AM16(A1_3, w71, xf[35]);
  STORE4(hp1, vl16, srdWS, sHo + rg * 2048 + 0, 1024);
  SB;
  SB; asm volatile("s_waitcnt vmcnt(12)\n\ts_barrier" :  :: "memory"); SB;
  AM16N(A0_0, w72, xf[0], w73); SB;
  s16x8 w75 = TR2(ldsB1, 4096, 4608);
  SB; AM16(A0_1, w72, xf[18]);
  SB;
  AM16(A0_2, w73, xf[0]); SB;
  s16x8 w76 = TR2(ldsA1, 8192, 8704);
  float g1_0a = A1_0[0] * A1_0[0];
  float g1_0b = A1_0[1] * A1_0[1];
  SB; AM16(A0_3, w73, xf[18]);
  g1_0a = __builtin_fmaf(g1_0a, GB, GA);
  g1_0b = __builtin_fmaf(g1_0b, GB, GA);
  g1_0a = g1_0a * A1_0[0];
  SB;
  AM16N(A0_0, w74, xf[1], w75); SB;
  s16x8 w77 = TR2(ldsB1, 8192, 8704);
  g1_0b = g1_0b * A1_0[1];
  SB; AM16(A0_1, w74, xf[19]);
  g1_0a = __builtin_amdgcn_exp2f(g1_0a);
  g1_0b = __builtin_amdgcn_exp2f(g1_0b);
  DMA2(lW, 24576, voffWa, srdW, sW, 3539328);
  SB;
  AM16(A0_2, w75, xf[1]); SB;
  s16x8 w78 = TR2(ldsA1, 12288, 12800);
  g1_0a = g1_0a + 1.0f;
  SB; AM16(A0_3, w75, xf[19]);
  g1_0b = g1_0b + 1.0f;
  g1_0a = __builtin_amdgcn_rcpf(g1_0a);
  SB;
  AM16N(A0_0, w76, xf[2], w77); SB;
  s16x8 w79 = TR2(ldsB1, 12288, 12800);
  g1_0b = __builtin_amdgcn_rcpf(g1_0b);
  SB; AM16(A0_1, w76, xf[20]);
  g1_0a = g1_0a * A1_0[0];
  g1_0b = g1_0b * A1_0[1];
  SB;
  AM16(A0_2, w77, xf[2]); SB;
  s16x8 w80 = TR2(ldsA1, 16384, 16896);
  hp0[0] = cvtpk(g1_0a, g1_0b);
  float g1_1a = A1_0[2] * A1_0[2];
  SB; AM16(A0_3, w77, xf[20]);
  float g1_1b = A1_0[3] * A1_0[3];
  g1_1a = __builtin_fmaf(g1_1a, GB, GA);
  g1_1b = __builtin_fmaf(g1_1b, GB, GA);
  DMA2(lW, 25600, voffWb, srdW, sW, 3613056);
  SB;
  AM16N(A0_0, w78, xf[3], w79); SB;
  s16x8 w81 = TR2(ldsB1, 16384, 16896);
  g1_1a = g1_1a * A1_0[2];
  SB; AM16(A0_1, w78, xf[21]);
  g1_1b = g1_1b * A1_0[3];
  g1_1a = __builtin_amdgcn_exp2f(g1_1a);
  SB;
  AM16(A0_2, w79, xf[3]); SB;
  s16x8 w82 = TR2(ldsA1, 20480, 20992);
  g1_1b = __builtin_amdgcn_exp2f(g1_1b);
  SB; AM16(A0_3, w79, xf[21]);
  g1_1a = g1_1a + 1.0f;
  g1_1b = g1_1b + 1.0f;
  DMA2(lW, 26624, voffWa, srdW, sW, 3686784);
  SB;
  AM16N(A0_0, w80, xf[4], w81); SB;
  s16x8 w83 = TR2(ldsB1, 20480, 20992);
  g1_1a = __builtin_amdgcn_rcpf(g1_1a);
  SB; AM16(A0_1, w80, xf[22]);
  g1_1b = __builtin_amdgcn_rcpf(g1_1b);
  g1_1a = g1_1a * A1_0[2];
  SB;
  AM16(A0_2, w81, xf[4]); SB;
  s16x8 w84 = TR2(ldsA1, 24576, 25088);
  g1_1b = g1_1b * A1_0[3];
  hp0[1] = cvtpk(g1_1a, g1_1b);
  A1_0 = *(volatile LAS f32x4*)(size_t)(ldsBias + 768);
  SB; AM16(A0_3, w81, xf[22]);
  float g1_2a = A1_2[0] * A1_2[0];
  float g1_2b = A1_2[1] * A1_2[1];
  g1_2a = __builtin_fmaf(g1_2a, GB, GA);
  SB;
  AM16N(A0_0, w82, xf[5], w83); SB;
  s16x8 w85 = TR2(ldsB1, 24576, 25088);
  g1_2b = __builtin_fmaf(g1_2b, GB, GA);
  g1_2a = g1_2a * A1_2[0];
  SB; AM16(A0_1, w82, xf[23]);
  g1_2b = g1_2b * A1_2[1];
  g1_2a = __builtin_amdgcn_exp2f(g1_2a);
  LOAD1K(pf2, pfo(ln, 4), srdO, pw8 * 4 * 128);
  SB;
  AM16(A0_2, w83, xf[5]); SB;
  s16x8 w86 = TR2(ldsA1, 28672, 29184);
  g1_2b = __builtin_amdgcn_exp2f(g1_2b);
  SB; AM16(A0_3, w83, xf[23]);
  g1_2a = g1_2a + 1.0f;
  g1_2b = g1_2b + 1.0f;
  SB;
  SB; asm volatile("s_waitcnt vmcnt(4)\n\ts_barrier" :  :: "memory"); SB;
  FLAG(sHF, 1);
  AM16N(A0_0, w84, xf[6], w85); SB;
  s16x8 w87 = TR2(ldsB1, 28672, 29184);
  g1_2a = __builtin_amdgcn_rcpf(g1_2a);
  SB; AM16(A0_1, w84, xf[24]);
  g1_2b = __builtin_amdgcn_rcpf(g1_2b);
  SB;
  AM16(A0_2, w85, xf[6]); SB;
  s16x8 w88 = TR2(ldsA1, 32768, 33280);
  g1_2a = g1_2a * A1_2[0];
  g1_2b = g1_2b * A1_2[1];
  SB; AM16(A0_3, w85, xf[24]);
  hp0[2] = cvtpk(g1_2a, g1_2b);
  float g1_3a = A1_2[2] * A1_2[2];
  float g1_3b = A1_2[3] * A1_2[3];
  SB;
  AM16N(A0_0, w86, xf[7], w87); SB;
  s16x8 w89 = TR2(ldsB1, 32768, 33280);
  g1_3a = __builtin_fmaf(g1_3a, GB, GA);
  g1_3b = __builtin_fmaf(g1_3b, GB, GA);
  SB; AM16(A0_1, w86, xf[25]);
  g1_3a = g1_3a * A1_2[2];
  g1_3b = g1_3b * A1_2[3];
  DMA2(lW, 49152, voffWa, srdW, sW, 512);
  SB;
  AM16(A0_2, w87, xf[7]); SB;
  s16x8 w90 = TR2(ldsA1, 36864, 37376);
  g1_3a = __builtin_amdgcn_exp2f(g1_3a);
  SB; AM16(A0_3, w87, xf[25]);
  g1_3b = __builtin_amdgcn_exp2f(g1_3b);
  g1_3a = g1_3a + 1.0f;
  SB;
  AM16N(A0_0, w88, xf[8], w89); SB;
  s16x8 w91 = TR2(ldsB1, 36864, 37376);
  g1_3b = g1_3b + 1.0f;
  SB; AM16(A0_1, w88, xf[26]);
  g1_3a = __builtin_amdgcn_rcpf(g1_3a);
  g1_3b = __builtin_amdgcn_rcpf(g1_3b);
  SB;
  AM16(A0_2, w89, xf[8]); SB;
  s16x8 w92 = TR2(ldsA1, 40960, 41472);
  g1_3a = g1_3a * A1_2[2];
  g1_3b = g1_3b * A1_2[3];
  SB; AM16(A0_3, w89, xf[26]);
  hp0[3] = cvtpk(g1_3a, g1_3b);
  A1_2 = *(volatile LAS f32x4*)(size_t)(ldsBias + 832);
  SB;
  AM16N(A0_0, w90, xf[9], w91); SB;
  s16x8 w93 = TR2(ldsB1, 40960, 41472);
  STORE4(hp0, vl16, srdWS, sHo + rg * 2048 + 16384, 0);
  float g1_4a = A1_1[0] * A1_1[0];
  float g1_4b = A1_1[1] * A1_1[1];
  SB; AM16(A0_1, w90, xf[27]);
  g1_4a = __builtin_fmaf(g1_4a, GB, GA);
  g1_4b = __builtin_fmaf(g1_4b, GB, GA);
  g1_4a = g1_4a * A1_1[0];
  DMA2(lW, 50176, voffWb, srdW, sW, 74240);
  SB;
  AM16(A0_2, w91, xf[9]); SB;
  s16x8 w94 = TR2(ldsA1, 45056, 45568);
  g1_4b = g1_4b * A1_1[1];
  SB; AM16(A0_3, w91, xf[27]);
  g1_4a = __builtin_amdgcn_exp2f(g1_4a);
  g1_4b = __builtin_amdgcn_exp2f(g1_4b);
  SB;
  AM16N(A0_0, w92, xf[10], w93); SB;
  s16x8 w95 = TR2(ldsB1, 45056, 45568);
  g1_4a = g1_4a + 1.0f;
  SB; AM16(A0_1, w92, xf[28]);
  g1_4b = g1_4b + 1.0f;
  g1_4a = __builtin_amdgcn_rcpf(g1_4a);
  SB;
  AM16(A0_2, w93, xf[10]); SB;
  s16x8 w96 = TR2(ldsA2, 0, 512);
  g1_4b = __builtin_amdgcn_rcpf(g1_4b);
  SB; AM16(A0_3, w93, xf[28]);
  g1_4a = g1_4a * A1_1[0];
  g1_4b = g1_4b * A1_1[1];
  SB;
  AM16N(A0_0, w94, xf[11], w95); SB;
  s16x8 w97 = TR2(ldsB2, 0, 512);
  hp1[0] = cvtpk(g1_4a, g1_4b);
  float g1_5a = A1_1[2] * A1_1[2];
  SB; AM16(A0_1, w94, xf[29]);
  float g1_5b = A1_1[3] * A1_1[3];
  g1_5a = __builtin_fmaf(g1_5a, GB, GA);
  g1_5b = __builtin_fmaf(g1_5b, GB, GA);
  DMA2(lW, 51200, voffWa, srdW, sW, 147968);
  SB;
  AM16(A0_2, w95, xf[11]); SB;
  s16x8 w98 = TR2(ldsA2, 4096, 4608);
  g1_5a = g1_5a * A1_1[2];
  SB; AM16(A0_3, w95, xf[29]);
  g1_5b = g1_5b * A1_1[3];
  g1_5a = __builtin_amdgcn_exp2f(g1_5a);
  SB;
  SB; asm volatile("s_barrier" :  :: "memory"); SB;
  AM16N(A0_0, w96, xf[12], w97); SB;
  s16x8 w99 = TR2(ldsB2, 4096, 4608);
  g1_5b = __builtin_amdgcn_exp2f(g1_5b);
  SB; AM16(A0_1, w96, xf[30]);
  g1_5a = g1_5a + 1.0f;
  g1_5b = g1_5b + 1.0f;
  SB;
  AM16(A0_2, w97, xf[12]); SB;
  s16x8 w100 = TR2(ldsA2, 8192, 8704);
  g1_5a = __builtin_amdgcn_rcpf(g1_5a);
  SB; AM16(A0_3, w97, xf[30]);
  g1_5b = __builtin_amdgcn_rcpf(g1_5b);
  g1_5a = g1_5a * A1_1[2];
  SB;
  AM16N(A0_0, w98, xf[13], w99); SB;
  s16x8 w101 = TR2(ldsB2, 8192, 8704);
  g1_5b = g1_5b * A1_1[3];
  hp1[1] = cvtpk(g1_5a, g1_5b);
  A1_1 = *(volatile LAS f32x4*)(size_t)(ldsBias + 768);
  SB; AM16(A0_1, w98, xf[31]);
  float g1_6a = A1_3[0] * A1_3[0];
  float g1_6b = A1_3[1] * A1_3[1];
  g1_6a = __builtin_fmaf(g1_6a, GB, GA);
  DMA2(lW, 73728, voffWa, srdW, sW, 1769984);
  SB;
  AM16(A0_2, w99, xf[13]); SB;
  s16x8 w102 = TR2(ldsA2, 12288, 12800);
  g1_6b = __builtin_fmaf(g1_6b, GB, GA);
  g1_6a = g1_6a * A1_3[0];
  SB; AM16(A0_3, w99, xf[31]);
  g1_6b = g1_6b * A1_3[1];
  g1_6a = __builtin_amdgcn_exp2f(g1_6a);
  SB;
  AM16N(A0_0, w100, xf[14], w101); SB;
  s16x8 w103 = TR2(ldsB2, 12288, 12800);
  g1_6b = __builtin_amdgcn_exp2f(g1_6b);
  SB; AM16(A0_1, w100, xf[32]);
  g1_6a = g1_6a + 1.0f;
  g1_6b = g1_6b + 1.0f;
  SB;
  AM16(A0_2, w101, xf[14]); SB;
  s16x8 w104 = TR2(ldsA2, 16384, 16896);
  g1_6a = __builtin_amdgcn_rcpf(g1_6a);
  SB; AM16(A0_3, w101, xf[32]);
  g1_6b = __builtin_amdgcn_rcpf(g1_6b);
  SB;
  AM16N(A0_0, w102, xf[15], w103); SB;
  s16x8 w105 = TR2(ldsB2, 16384, 16896);
  g1_6a = g1_6a * A1_3[0];
  g1_6b = g1_6b * A1_3[1];
  SB; AM16(A0_1, w102, xf[33]);
  hp1[2] = cvtpk(g1_6a, g1_6b);
  float g1_7a = A1_3[2] * A1_3[2];
  float g1_7b = A1_3[3] * A1_3[3];
  DMA2(lW, 74752, voffWb, srdW, sW, 1843712);
  SB;
  AM16(A0_2, w103, xf[15]); SB;
  s16x8 w106 = TR2(ldsA2, 20480, 20992);
  g1_7a = __builtin_fmaf(g1_7a, GB, GA);
  g1_7b = __builtin_fmaf(g1_7b, GB, GA);
  SB; AM16(A0_3, w103, xf[33]);
  g1_7a = g1_7a * A1_3[2];
  g1_7b = g1_7b * A1_3[3];
  SB;
  AM16N(A0_0, w104, xf[16], w105); SB;
  s16x8 w107 = TR2(ldsB2, 20480, 20992);
  g1_7a = __builtin_amdgcn_exp2f(g1_7a);
  SB; AM16(A0_1, w104, xf[34]);
  g1_7b = __builtin_amdgcn_exp2f(g1_7b);
  g1_7a = g1_7a + 1.0f;
  SB;
  AM16(A0_2, w105, xf[16]); SB;
  s16x8 w108 = TR2(ldsA2, 24576, 25088);
  g1_7b = g1_7b + 1.0f;
  SB; AM16(A0_3, w105, xf[34]);
  g1_7a = __builtin_amdgcn_rcpf(g1_7a);
  g1_7b = __builtin_amdgcn_rcpf(g1_7b);
  SB;
  AM16N(A0_0, w106, xf[17], w107); SB;
  s16x8 w109 = TR2(ldsB2, 24576, 25088);
  g1_7a = g1_7a * A1_3[2];
  g1_7b = g1_7b * A1_3[3];
  SB; AM16(A0_1, w106, xf[35]);
  hp1[3] = cvtpk(g1_7a, g1_7b);
  A1_3 = *(volatile LAS f32x4*)(size_t)(ldsBias + 832);
  DMA2(lW, 75776, voffWa, srdW, sW, 1917440);
  SB;
  AM16(A0_2, w107, xf[17]); SB;
  s16x8 w110 = TR2(ldsA2, 28672, 29184);
  SB; AM16(A0_3, w107, xf[35]);
  STORE4(hp1, vl16, srdWS, sHo + rg * 2048 + 16384, 1024);
  SB;
  SB; asm volatile("s_barrier" :  :: "memory"); SB;
  AM16N(A1_0, w108, xf[0], w109); SB;
  s16x8 w111 = TR2(ldsB2, 28672, 29184);
  SB; AM16(A1_1, w108, xf[18]);
  SB;
  AM16(A1_2, w109, xf[0]); SB;
  s16x8 w112 = TR2(ldsA2, 32768, 33280);
  float g2_0a = A0_0[0] * A0_0[0];
  float g2_0b = A0_0[1] * A0_0[1];
  SB; AM16(A1_3, w109, xf[18]);
  g2_0a = __builtin_fmaf(g2_0a, GB, GA);
  g2_0b = __builtin_fmaf(g2_0b, GB, GA);
  g2_0a = g2_0a * A0_0[0];
  SB;
  AM16N(A1_0, w110, xf[1], w111); SB;
  s16x8 w113 = TR2(ldsB2, 32768, 33280);
  g2_0b = g2_0b * A0_0[1];
  SB; AM16(A1_1, w110, xf[19]);
  g2_0a = __builtin_amdgcn_exp2f(g2_0a);
  g2_0b = __builtin_amdgcn_exp2f(g2_0b);
  DMA2(lW, 98304, voffWa, srdW, sW, 3539456);
  SB;
  AM16(A1_2, w111, xf[1]); SB;
  s16x8 w114 = TR2(ldsA2, 36864, 37376);
  g2_0a = g2_0a + 1.0f;
  SB; AM16(A1_3, w111, xf[19]);
  g2_0b = g2_0b + 1.0f;
  g2_0a = __builtin_amdgcn_rcpf(g2_0a);
  SB;
  AM16N(A1_0, w112, xf[2], w113); SB;
  s16x8 w115 = TR2(ldsB2, 36864, 37376);
  g2_0b = __builtin_amdgcn_rcpf(g2_0b);
  SB; AM16(A1_1, w112, xf[20]);
  g2_0a = g2_0a * A0_0[0];
  g2_0b = g2_0b * A0_0[1];
  SB;
  AM16(A1_2, w113, xf[2]); SB;
  s16x8 w116 = TR2(ldsA2, 40960, 41472);
  hp0[0] = cvtpk(g2_0a, g2_0b);
  float g2_1a = A0_0[2] * A0_0[2];
  SB; AM16(A1_3, w113, xf[20]);
  float g2_1b = A0_0[3] * A0_0[3];
  g2_1a = __builtin_fmaf(g2_1a, GB, GA);
  g2_1b = __builtin_fmaf(g2_1b, GB, GA);
  SB;
  AM16N(A1_0, w114, xf[3], w115); SB;
  s16x8 w117 = TR2(ldsB2, 40960, 41472);
  g2_1a = g2_1a * A0_0[2];
  SB; AM16(A1_1, w114, xf[21]);
  g2_1b = g2_1b * A0_0[3];
  g2_1a = __builtin_amdgcn_exp2f(g2_1a);
  DMA2(lW, 99328, voffWb, srdW, sW, 3613184);
  SB;
  AM16(A1_2, w115, xf[3]); SB;
  s16x8 w118 = TR2(ldsA2, 45056, 45568);
  g2_1b = __builtin_amdgcn_exp2f(g2_1b);
  SB; AM16(A1_3, w115, xf[21]);
  g2_1a = g2_1a + 1.0f;
  g2_1b = g2_1b + 1.0f;
  SB;
  AM16N(A1_0, w116, xf[4], w117); SB;
  s16x8 w119 = TR2(ldsB2, 45056, 45568);
  g2_1a = __builtin_amdgcn_rcpf(g2_1a);
  SB; AM16(A1_1, w116, xf[22]);
  g2_1b = __builtin_amdgcn_rcpf(g2_1b);
  g2_1a = g2_1a * A0_0[2];
  SB;
  AM16(A1_2, w117, xf[4]); SB;
  s16x8 w120 = TR2(ldsA0, 0, 512);
  g2_1b = g2_1b * A0_0[3];
  hp0[1] = cvtpk(g2_1a, g2_1b);
  A0_0 = *(volatile LAS f32x4*)(size_t)(ldsBias + 1024);
  SB; AM16(A1_3, w117, xf[22]);
  float g2_2a = A0_2[0] * A0_2[0];
  float g2_2b = A0_2[1] * A0_2[1];
  g2_2a = __builtin_fmaf(g2_2a, GB, GA);
  SB;
  AM16N(A1_0, w118, xf[5], w119); SB;
  s16x8 w121 = TR2(ldsB0, 0, 512);
  g2_2b = __builtin_fmaf(g2_2b, GB, GA);
  g2_2a = g2_2a * A0_2[0];
  SB; AM16(A1_1, w118, xf[23]);
  g2_2b = g2_2b * A0_2[1];
  g2_2a = __builtin_amdgcn_exp2f(g2_2a);
  DMA2(lW, 100352, voffWa, srdW, sW, 3686912);
  SB;
  AM16(A1_2, w119, xf[5]); SB;
  s16x8 w122 = TR2(ldsA0, 4096, 4608);
  g2_2b = __builtin_amdgcn_exp2f(g2_2b);
  SB; AM16(A1_3, w119, xf[23]);
  g2_2a = g2_2a + 1.0f;
  g2_2b = g2_2b + 1.0f;
  SB;
  SB; asm volatile("s_waitcnt vmcnt(3)\n\ts_barrier" :  :: "memory"); SB;
  FLAG(sHF, 2);
  AM16N(A1_0, w120, xf[6], w121); SB;
  s16x8 w123 = TR2(ldsB0, 4096, 4608);
  g2_2a = __builtin_amdgcn_rcpf(g2_2a);
  SB; AM16(A1_1, w120, xf[24]);
  g2_2b = __builtin_amdgcn_rcpf(g2_2b);
  SB;
  AM16(A1_2, w121, xf[6]); SB;
  s16x8 w124 = TR2(ldsA0, 8192, 8704);
  g2_2a = g2_2a * A0_2[0];
  g2_2b = g2_2b * A0_2[1];
  SB; AM16(A1_3, w121, xf[24]);
  hp0[2] = cvtpk(g2_2a, g2_2b);
  float g2_3a = A0_2[2] * A0_2[2];
  float g2_3b = A0_2[3] * A0_2[3];
  SB;
  AM16N(A1_0, w122, xf[7], w123); SB;
  s16x8 w125 = TR2(ldsB0, 8192, 8704);
  g2_3a = __builtin_fmaf(g2_3a, GB, GA);
  g2_3b = __builtin_fmaf(g2_3b, GB, GA);
  SB; AM16(A1_1, w122, xf[25]);
  g2_3a = g2_3a * A0_2[2];
  g2_3b = g2_3b * A0_2[3];
  DMA2(lW, 122880, voffWa, srdW, sW, 640);
  SB;
  AM16(A1_2, w123, xf[7]); SB;
  s16x8 w126 = TR2(ldsA0, 12288, 12800);
  g2_3a = __builtin_amdgcn_exp2f(g2_3a);
  SB; AM16(A1_3, w123, xf[25]);
  g2_3b = __builtin_amdgcn_exp2f(g2_3b);
  g2_3a = g2_3a + 1.0f;
  SB;
  AM16N(A1_0, w124, xf[8], w125); SB;
  s16x8 w127 = TR2(ldsB0, 12288, 12800);
  g2_3b = g2_3b + 1.0f;
  SB; AM16(A1_1, w124, xf[26]);
  g2_3a = __builtin_amdgcn_rcpf(g2_3a);
  g2_3b = __builtin_amdgcn_rcpf(g2_3b);
  SB;
  AM16(A1_2, w125, xf[8]); SB;
  s16x8 w128 = TR2(ldsA0, 16384, 16896);
  g2_3a = g2_3a * A0_2[2];
  g2_3b = g2_3b * A0_2[3];
  SB; AM16(A1_3, w125, xf[26]);
  hp0[3] = cvtpk(g2_3a, g2_3b);
  A0_2 = *(volatile LAS f32x4*)(size_t)(ldsBias + 1088);
  SB;
  AM16N(A1_0, w126, xf[9], w127); SB;
  s16x8 w129 = TR2(ldsB0, 16384, 16896);
  STORE4(hp0, vl16, srdWS, sHo + rg * 2048 + 32768, 0);
  float g2_4a = A0_1[0] * A0_1[0];
  float g2_4b = A0_1[1] * A0_1[1];
  SB; AM16(A1_1, w126, xf[27]);
  g2_4a = __builtin_fmaf(g2_4a, GB, GA);
  g2_4b = __builtin_fmaf(g2_4b, GB, GA);
  g2_4a = g2_4a * A0_1[0];
  DMA2(lW, 123904, voffWb, srdW, sW, 74368);
  SB;
  AM16(A1_2, w127, xf[9]); SB;
  s16x8 w130 = TR2(ldsA0, 20480, 20992);
  g2_4b = g2_4b * A0_1[1];
  SB; AM16(A1_3, w127, xf[27]);
  g2_4a = __builtin_amdgcn_exp2f(g2_4a);
  g2_4b = __builtin_amdgcn_exp2f(g2_4b);
  SB;
  AM16N(A1_0, w128, xf[10], w129); SB;
  s16x8 w131 = TR2(ldsB0, 20480, 20992);
  g2_4a = g2_4a + 1.0f;
  SB; AM16(A1_1, w128, xf[28]);
  g2_4b = g2_4b + 1.0f;
  g2_4a = __builtin_amdgcn_rcpf(g2_4a);
  SB;
  AM16(A1_2, w129, xf[10]); SB;
  s16x8 w132 = TR2(ldsA0, 24576, 25088);
  g2_4b = __builtin_amdgcn_rcpf(g2_4b);
  SB; AM16(A1_3, w129, xf[28]);
  g2_4a = g2_4a * A0_1[0];
  g2_4b = g2_4b * A0_1[1];
  SB;
  AM16N(A1_0, w130, xf[11], w131); SB;
  s16x8 w133 = TR2(ldsB0, 24576, 25088);
  hp1[0] = cvtpk(g2_4a, g2_4b);
  float g2_5a = A0_1[2] * A0_1[2];
  SB; AM16(A1_1, w130, xf[29]);
  float g2_5b = A0_1[3] * A0_1[3];
  g2_5a = __builtin_fmaf(g2_5a, GB, GA);
  g2_5b = __builtin_fmaf(g2_5b, GB, GA);
  DMA2(lW, 124928, voffWa, srdW, sW, 148096);
  SB;
  AM16(A1_2, w131, xf[11]); SB;
  s16x8 w134 = TR2(ldsA0, 28672, 29184);
  g2_5a = g2_5a * A0_1[2];
  SB; AM16(A1_3, w131, xf[29]);
  g2_5b = g2_5b * A0_1[3];
  g2_5a = __builtin_amdgcn_exp2f(g2_5a);
  SB;
  SB; asm volatile("s_barrier" :  :: "memory"); SB;
  AM16N(A1_0, w132, xf[12], w133); SB;
  s16x8 w135 = TR2(ldsB0, 28672, 29184);
  g2_5b = __builtin_amdgcn_exp2f(g2_5b);
  SB; AM16(A1_1, w132, xf[30]);
  g2_5a = g2_5a + 1.0f;
  g2_5b = g2_5b + 1.0f;
  SB;
  AM16(A1_2, w133, xf[12]); SB;
  s16x8 w136 = TR2(ldsA0, 32768, 33280);
  g2_5a = __builtin_amdgcn_rcpf(g2_5a);
  SB; AM16(A1_3, w133, xf[30]);
  g2_5b = __builtin_amdgcn_rcpf(g2_5b);
  g2_5a = g2_5a * A0_1[2];
  SB;
  AM16N(A1_0, w134, xf[13], w135); SB;
  s16x8 w137 = TR2(ldsB0, 32768, 33280);
  g2_5b = g2_5b * A0_1[3];
  hp1[1] = cvtpk(g2_5a, g2_5b);
  A0_1 = *(volatile LAS f32x4*)(size_t)(ldsBias + 1024);
  SB; AM16(A1_1, w134, xf[31]);
  float g2_6a = A0_3[0] * A0_3[0];
  float g2_6b = A0_3[1] * A0_3[1];
  g2_6a = __builtin_fmaf(g2_6a, GB, GA);
  DMA2(lW, 0, voffWa, srdW, sW, 1770112);
  SB;
  AM16(A1_2, w135, xf[13]); SB;
  s16x8 w138 = TR2(ldsA0, 36864, 37376);
  g2_6b = __builtin_fmaf(g2_6b, GB, GA);
  g2_6a = g2_6a * A0_3[0];
  SB; AM16(A1_3, w135, xf[31]);
  g2_6b = g2_6b * A0_3[1];
  g2_6a = __builtin_amdgcn_exp2f(g2_6a);
  SB;
  AM16N(A1_0, w136, xf[14], w137); SB;
  s16x8 w139 = TR2(ldsB0, 36864, 37376);
  g2_6b = __builtin_amdgcn_exp2f(g2_6b);
  SB; AM16(A1_1, w136, xf[32]);
  g2_6a = g2_6a + 1.0f;
  g2_6b = g2_6b + 1.0f;
  SB;
  AM16(A1_2, w137, xf[14]); SB;
  s16x8 w140 = TR2(ldsA0, 40960, 41472);
  g2_6a = __builtin_amdgcn_rcpf(g2_6a);
  SB; AM16(A1_3, w137, xf[32]);
  g2_6b = __builtin_amdgcn_rcpf(g2_6b);
  SB;
  AM16N(A1_0, w138, xf[15], w139); SB;
  s16x8 w141 = TR2(ldsB0, 40960, 41472);
  g2_6a = g2_6a * A0_3[0];
  g2_6b = g2_6b * A0_3[1];
  SB; AM16(A1_1, w138, xf[33]);
  hp1[2] = cvtpk(g2_6a, g2_6b);
  float g2_7a = A0_3[2] * A0_3[2];
  float g2_7b = A0_3[3] * A0_3[3];
  DMA2(lW, 1024, voffWb, srdW, sW, 1843840);
  SB;
  AM16(A1_2, w139, xf[15]); SB;
  s16x8 w142 = TR2(ldsA0, 45056, 45568);
  g2_7a = __builtin_fmaf(g2_7a, GB, GA);
  g2_7b = __builtin_fmaf(g2_7b, GB, GA);
  SB; AM16(A1_3, w139, xf[33]);
  g2_7a = g2_7a * A0_3[2];
  g2_7b = g2_7b * A0_3[3];
  SB;
  AM16N(A1_0, w140, xf[16], w141); SB;
  s16x8 w143 = TR2(ldsB0, 45056, 45568);
  g2_7a = __builtin_amdgcn_exp2f(g2_7a);
  SB; AM16(A1_1, w140, xf[34]);
  g2_7b = __builtin_amdgcn_exp2f(g2_7b);
  g2_7a = g2_7a + 1.0f;
  SB;
  AM16(A1_2, w141, xf[16]); SB;
  s16x8 w144 = TR2(ldsA1, 0, 512);
  g2_7b = g2_7b + 1.0f;
  SB; AM16(A1_3, w141, xf[34]);
  g2_7a = __builtin_amdgcn_rcpf(g2_7a);
  g2_7b = __builtin_amdgcn_rcpf(g2_7b);
  SB;
  AM16N(A1_0, w142, xf[17], w143); SB;
  s16x8 w145 = TR2(ldsB1, 0, 512);
  g2_7a = g2_7a * A0_3[2];
  g2_7b = g2_7b * A0_3[3];
  SB; AM16(A1_1, w142, xf[35]);
  hp1[3] = cvtpk(g2_7a, g2_7b);
  A0_3 = *(volatile LAS f32x4*)(size_t)(ldsBias + 1088);
  DMA2(lW, 2048, voffWa, srdW, sW, 1917568);
  SB;
  AM16(A1_2, w143, xf[17]); SB;
  s16x8 w146 = TR2(ldsA1, 4096, 4608);
  SB; AM16(A1_3, w143, xf[35]);
  STORE4(hp1, vl16, srdWS, sHo + rg * 2048 + 32768, 1024);
  SB;
  SB; asm volatile("s_barrier" :  :: "memory"); SB;
  AM16N(A0_0, w144, xf[0], w145); SB;
  s16x8 w147 = TR2(ldsB1, 4096, 4608);
  SB; AM16(A0_1, w144, xf[18]);
  SB;
  AM16(A0_2, w145, xf[0]); SB;
  s16x8 w148 = TR2(ldsA1, 8192, 8704);
  float g3_0a = A1_0[0] * A1_0[0];
  float g3_0b = A1_0[1] * A1_0[1];
  SB; AM16(A0_3, w145, xf[18]);
  g3_0a = __builtin_fmaf(g3_0a, GB, GA);
  g3_0b = __builtin_fmaf(g3_0b, GB, GA);
  g3_0a = g3_0a * A1_0[0];
  SB;
  AM16N(A0_0, w146, xf[1], w147); SB;
  s16x8 w149 = TR2(ldsB1, 8192, 8704);
  g3_0b = g3_0b * A1_0[1];
  SB; AM16(A0_1, w146, xf[19]);
  g3_0a = __builtin_amdgcn_exp2f(g3_0a);
  g3_0b = __builtin_amdgcn_exp2f(g3_0b);
  DMA2(lW, 24576, voffWa, srdW, sW, 3539584);
  SB;
  AM16(A0_2, w147, xf[1]); SB;
  s16x8 w150 = TR2(ldsA1, 12288, 12800);
  g3_0a = g3_0a + 1.0f;
  SB; AM16(A0_3, w147, xf[19]);
  g3_0b = g3_0b + 1.0f;
  g3_0a = __builtin_amdgcn_rcpf(g3_0a);
  SB;
  AM16N(A0_0, w148, xf[2], w149); SB;
  s16x8 w151 = TR2(ldsB1, 12288, 12800);
  g3_0b = __builtin_amdgcn_rcpf(g3_0b);
  SB; AM16(A0_1, w148, xf[20]);
  g3_0a = g3_0a * A1_0[0];
  g3_0b = g3_0b * A1_0[1];
  SB;
  AM16(A0_2, w149, xf[2]); SB;
  s16x8 w152 = TR2(ldsA1, 16384, 16896);
  hp0[0] = cvtpk(g3_0a, g3_0b);
  float g3_1a = A1_0[2] * A1_0[2];
  SB; AM16(A0_3, w149, xf[20]);
  float g3_1b = A1_0[3] * A1_0[3];
  g3_1a = __builtin_fmaf(g3_1a, GB, GA);
  g3_1b = __builtin_fmaf(g3_1b, GB, GA);
  SB;
  AM16N(A0_0, w150, xf[3], w151); SB;
  s16x8 w153 = TR2(ldsB1, 16384, 16896);
  g3_1a = g3_1a * A1_0[2];
  SB; AM16(A0_1, w150, xf[21]);
  g3_1b = g3_1b * A1_0[3];
  g3_1a = __builtin_amdgcn_exp2f(g3_1a);
  DMA2(lW, 25600, voffWb, srdW, sW, 3613312);
  SB;
  AM16(A0_2, w151, xf[3]); SB;
  s16x8 w154 = TR2(ldsA1, 20480, 20992);
  g3_1b = __builtin_amdgcn_exp2f(g3_1b);
  SB; AM16(A0_3, w151, xf[21]);
  g3_1a = g3_1a + 1.0f;
  g3_1b = g3_1b + 1.0f;
  SB;
  AM16N(A0_0, w152, xf[4], w153); SB;
  s16x8 w155 = TR2(ldsB1, 20480, 20992);
  g3_1a = __builtin_amdgcn_rcpf(g3_1a);
  SB; AM16(A0_1, w152, xf[22]);
  g3_1b = __builtin_amdgcn_rcpf(g3_1b);
  g3_1a = g3_1a * A1_0[2];
  SB;
  AM16(A0_2, w153, xf[4]); SB;
  s16x8 w156 = TR2(ldsA1, 24576, 25088);
  g3_1b = g3_1b * A1_0[3];
  hp0[1] = cvtpk(g3_1a, g3_1b);
  A1_0 = *(volatile LAS f32x4*)(size_t)(ldsBias + 1280);
  SB; AM16(A0_3, w153, xf[22]);
  float g3_2a = A1_2[0] * A1_2[0];
  float g3_2b = A1_2[1] * A1_2[1];
  g3_2a = __builtin_fmaf(g3_2a, GB, GA);
  SB;
  AM16N(A0_0, w154, xf[5], w155); SB;
  s16x8 w157 = TR2(ldsB1, 24576, 25088);
  g3_2b = __builtin_fmaf(g3_2b, GB, GA);
  g3_2a = g3_2a * A1_2[0];
  SB; AM16(A0_1, w154, xf[23]);
  g3_2b = g3_2b * A1_2[1];
  g3_2a = __builtin_amdgcn_exp2f(g3_2a);
  DMA2(lW, 26624, voffWa, srdW, sW, 3687040);
  SB;
  AM16(A0_2, w155, xf[5]); SB;
  s16x8 w158 = TR2(ldsA1, 28672, 29184);
  g3_2b = __builtin_amdgcn_exp2f(g3_2b);
  SB; AM16(A0_3, w155, xf[23]);
  g3_2a = g3_2a + 1.0f;
  g3_2b = g3_2b + 1.0f;
  SB;
  SB; asm volatile("s_waitcnt vmcnt(3)\n\ts_barrier" : "+v"(pf0), "+v"(pf2) :: "memory"); SB;
  FLAG(sHF, 3);
  AM16N(A0_0, w156, xf[6], w157); SB;
  s16x8 w159 = TR2(ldsB1, 28672, 29184);
  g3_2a = __builtin_amdgcn_rcpf(g3_2a);
  SB; AM16(A0_1, w156, xf[24]);
  g3_2b = __builtin_amdgcn_rcpf(g3_2b);
  SB;
  AM16(A0_2, w157, xf[6]); SB;
  s16x8 w160 = TR2(ldsA1, 32768, 33280);
  g3_2a = g3_2a * A1_2[0];
  g3_2b = g3_2b * A1_2[1];
  SB; AM16(A0_3, w157, xf[24]);
  hp0[2] = cvtpk(g3_2a, g3_2b);
  float g3_3a = A1_2[2] * A1_2[2];
  float g3_3b = A1_2[3] * A1_2[3];
  SB;
  AM16N(A0_0, w158, xf[7], w159); SB;
  s16x8 w161 = TR2(ldsB1, 32768, 33280);
  g3_3a = __builtin_fmaf(g3_3a, GB, GA);
  g3_3b = __builtin_fmaf(g3_3b, GB, GA);
  SB; AM16(A0_1, w158, xf[25]);
  g3_3a = g3_3a * A1_2[2];
  g3_3b = g3_3b * A1_2[3];
  DMA2(lW, 49152, voffV0, srdW2, sV, 0);
  SB;
  AM16(A0_2, w159, xf[7]); SB;
  s16x8 w162 = TR2(ldsA1, 36864, 37376);
  g3_3a = __builtin_amdgcn_exp2f(g3_3a);
  SB; AM16(A0_3, w159, xf[25]);
  g3_3b = __builtin_amdgcn_exp2f(g3_3b);
  g3_3a = g3_3a + 1.0f;
  SB;
  AM16N(A0_0, w160, xf[8], w161); SB;
  s16x8 w163 = TR2(ldsB1, 36864, 37376);
  g3_3b = g3_3b + 1.0f;
  SB; AM16(A0_1, w160, xf[26]);
  g3_3a = __builtin_amdgcn_rcpf(g3_3a);
  g3_3b = __builtin_amdgcn_rcpf(g3_3b);
  SB;
  AM16(A0_2, w161, xf[8]); SB;
  s16x8 w164 = TR2(ldsA1, 40960, 41472);
  g3_3a = g3_3a * A1_2[2];
  g3_3b = g3_3b * A1_2[3];
  SB; AM16(A0_3, w161, xf[26]);
  hp0[3] = cvtpk(g3_3a, g3_3b);
  A1_2 = *(volatile LAS f32x4*)(size_t)(ldsBias + 1344);
  SB;
  AM16N(A0_0, w162, xf[9], w163); SB;
  s16x8 w165 = TR2(ldsB1, 40960, 41472);
  STORE4(hp0, vl16, srdWS, sHo + rg * 2048 + 49152, 0);
  float g3_4a = A1_1[0] * A1_1[0];
  float g3_4b = A1_1[1] * A1_1[1];
  SB; AM16(A0_1, w162, xf[27]);
  g3_4a = __builtin_fmaf(g3_4a, GB, GA);
  g3_4b = __builtin_fmaf(g3_4b, GB, GA);
  g3_4a = g3_4a * A1_1[0];
  DMA2(lW, 50176, voffV1, srdW2, sV, 0);
  SB;
  AM16(A0_2, w163, xf[9]); SB;
  s16x8 w166 = TR2(ldsA1, 45056, 45568);
  g3_4b = g3_4b * A1_1[1];
  SB; AM16(A0_3, w163, xf[27]);
  g3_4a = __builtin_amdgcn_exp2f(g3_4a);
  g3_4b = __builtin_amdgcn_exp2f(g3_4b);
  SB;
  AM16N(A0_0, w164, xf[10], w165); SB;
  s16x8 w167 = TR2(ldsB1, 45056, 45568);
  g3_4a = g3_4a + 1.0f;
  SB; AM16(A0_1, w164, xf[28]);
  g3_4b = g3_4b + 1.0f;
  g3_4a = __builtin_amdgcn_rcpf(g3_4a);
  SB;
  AM16(A0_2, w165, xf[10]); SB;
  s16x8 w168 = TR2(ldsA2, 0, 512);
  g3_4b = __builtin_amdgcn_rcpf(g3_4b);
  SB; AM16(A0_3, w165, xf[28]);
  g3_4a = g3_4a * A1_1[0];
  g3_4b = g3_4b * A1_1[1];
  SB;
  AM16N(A0_0, w166, xf[11], w167); SB;
  s16x8 w169 = TR2(ldsB2, 0, 512);
  hp1[0] = cvtpk(g3_4a, g3_4b);
  float g3_5a = A1_1[2] * A1_1[2];
  SB; AM16(A0_1, w166, xf[29]);
  float g3_5b = A1_1[3] * A1_1[3];
  g3_5a = __builtin_fmaf(g3_5a, GB, GA);
  g3_5b = __builtin_fmaf(g3_5b, GB, GA);
  DMA2(lW, 51200, voffV2, srdW2, sV, 0);
  SB;
  AM16(A0_2, w167, xf[11]); SB;
  s16x8 w170 = TR2(ldsA2, 4096, 4608);
  g3_5a = g3_5a * A1_1[2];
  SB; AM16(A0_3, w167, xf[29]);
  g3_5b = g3_5b * A1_1[3];
  g3_5a = __builtin_amdgcn_exp2f(g3_5a);
  SB;
  SB; asm volatile("s_barrier" :  :: "memory"); SB;
  u32x2 pl0;
  LOAD2(pl0, voffHP, srdWS, 0, 0, " sc1");
  AM16N(A0_0, w168, xf[12], w169); SB;
  s16x8 w171 = TR2(ldsB2, 4096, 4608);
  g3_5b = __builtin_amdgcn_exp2f(g3_5b);
  SB; AM16(A0_1, w168, xf[30]);
  g3_5a = g3_5a + 1.0f;
  g3_5b = g3_5b + 1.0f;
  SB;
  AM16(A0_2, w169, xf[12]); SB;
  s16x8 w172 = TR2(ldsA2, 8192, 8704);
  g3_5a = __builtin_amdgcn_rcpf(g3_5a);
  SB; AM16(A0_3, w169, xf[30]);
  g3_5b = __builtin_amdgcn_rcpf(g3_5b);
  g3_5a = g3_5a * A1_1[2];
  SB;
  AM16N(A0_0, w170, xf[13], w171); SB;
  s16x8 w173 = TR2(ldsB2, 8192, 8704);
  g3_5b = g3_5b * A1_1[3];
  hp1[1] = cvtpk(g3_5a, g3_5b);
  A1_1 = *(volatile LAS f32x4*)(size_t)(ldsBias + 1280);
  SB; AM16(A0_1, w170, xf[31]);
  float g3_6a = A1_3[0] * A1_3[0];
  float g3_6b = A1_3[1] * A1_3[1];
  g3_6a = __builtin_fmaf(g3_6a, GB, GA);
  DMA2(lW, 73728, voffV0, srdW2, sV, 442368);
  SB;
  AM16(A0_2, w171, xf[13]); SB;
  s16x8 w174 = TR2(ldsA2, 12288, 12800);
  g3_6b = __builtin_fmaf(g3_6b, GB, GA);
  g3_6a = g3_6a * A1_3[0];
  SB; AM16(A0_3, w171, xf[31]);
  g3_6b = g3_6b * A1_3[1];
  g3_6a = __builtin_amdgcn_exp2f(g3_6a);
  SB;
  AM16N(A0_0, w172, xf[14], w173); SB;
  s16x8 w175 = TR2(ldsB2, 12288, 12800);
  g3_6b = __builtin_amdgcn_exp2f(g3_6b);
  SB; AM16(A0_1, w172, xf[32]);
  g3_6a = g3_6a + 1.0f;
  g3_6b = g3_6b + 1.0f;
  SB;
  AM16(A0_2, w173, xf[14]); SB;
  s16x8 w176 = TR2(ldsA2, 16384, 16896);
  g3_6a = __builtin_amdgcn_rcpf(g3_6a);
  SB; AM16(A0_3, w173, xf[32]);
  g3_6b = __builtin_amdgcn_rcpf(g3_6b);
  SB;
  AM16N(A0_0, w174, xf[15], w175); SB;
  s16x8 w177 = TR2(ldsB2, 16384, 16896);
  g3_6a = g3_6a * A1_3[0];
  g3_6b = g3_6b * A1_3[1];
  SB; AM16(A0_1, w174, xf[33]);
  hp1[2] = cvtpk(g3_6a, g3_6b);
  float g3_7a = A1_3[2] * A1_3[2];
  float g3_7b = A1_3[3] * A1_3[3];
  DMA2(lW, 74752, voffV1, srdW2, sV, 442368);
  SB;
  AM16(A0_2, w175, xf[15]); SB;
  s16x8 w178 = TR2(ldsA2, 20480, 20992);
  g3_7a = __builtin_fmaf(g3_7a, GB, GA);
  g3_7b = __builtin_fmaf(g3_7b, GB, GA);
  SB; AM16(A0_3, w175, xf[33]);
  g3_7a = g3_7a * A1_3[2];
  g3_7b = g3_7b * A1_3[3];
  SB;
  AM16N(A0_0, w176, xf[16], w177); SB;
  s16x8 w179 = TR2(ldsB2, 20480, 20992);
  g3_7a = __builtin_amdgcn_exp2f(g3_7a);
  SB; AM16(A0_1, w176, xf[34]);
  g3_7b = __builtin_amdgcn_exp2f(g3_7b);
  g3_7a = g3_7a + 1.0f;
  SB;
  AM16(A0_2, w177, xf[16]); SB;
  s16x8 w180 = TR2(ldsA2, 24576, 25088);
  g3_7b = g3_7b + 1.0f;
  SB; AM16(A0_3, w177, xf[34]);
  g3_7a = __builtin_amdgcn_rcpf(g3_7a);
  g3_7b = __builtin_amdgcn_rcpf(g3_7b);
  SB;
  AM16N(A0_0, w178, xf[17], w179); SB;
  s16x8 w181 = TR2(ldsB2, 24576, 25088);
  g3_7a = g3_7a * A1_3[2];
  g3_7b = g3_7b * A1_3[3];
  SB; AM16(A0_1, w178, xf[35]);
  hp1[3] = cvtpk(g3_7a, g3_7b);
  A1_3 = *(volatile LAS f32x4*)(size_t)(ldsBias + 1344);
  DMA2(lW, 75776, voffV2, srdW2, sV, 442368);
  SB;
  AM16(A0_2, w179, xf[17]); SB;
  s16x8 w182 = TR2(ldsA2, 28672, 29184);
  SB; AM16(A0_3, w179, xf[35]);
  STORE4(hp1, vl16, srdWS, sHo + rg * 2048 + 49152, 1024);
  SB;
  SB; asm volatile("s_barrier" :  :: "memory"); SB;
  AM16N(A1_0, w180, xf[0], w181); SB;
  s16x8 w183 = TR2(ldsB2, 28672, 29184);
  SB; AM16(A1_1, w180, xf[18]);
  SB;
  AM16(A1_2, w181, xf[0]); SB;
  s16x8 w184 = TR2(ldsA2, 32768, 33280);
  float g4_0a = A0_0[0] * A0_0[0];
  float g4_0b = A0_0[1] * A0_0[1];
  SB; AM16(A1_3, w181, xf[18]);
  g4_0a = __builtin_fmaf(g4_0a, GB, GA);
  g4_0b = __builtin_fmaf(g4_0b, GB, GA);
  SB;
  AM16N(A1_0, w182, xf[1], w183); SB;
  s16x8 w185 = TR2(ldsB2, 32768, 33280);
  g4_0a = g4_0a * A0_0[0];
  SB; AM16(A1_1, w182, xf[19]);
  g4_0b = g4_0b * A0_0[1];
  g4_0a = __builtin_amdgcn_exp2f(g4_0a);
  DMA2(lW, 98304, voffV0, srdW2, sV, 884736);
  SB;
  AM16(A1_2, w183, xf[1]); SB;
  s16x8 w186 = TR2(ldsA2, 36864, 37376);
  g4_0b = __builtin_amdgcn_exp2f(g4_0b);
  g4_0a = g4_0a + 1.0f;
  SB; AM16(A1_3, w183, xf[19]);
  g4_0b = g4_0b + 1.0f;
  g4_0a = __builtin_amdgcn_rcpf(g4_0a);
  SB;
  AM16N(A1_0, w184, xf[2], w185); SB;
  s16x8 w187 = TR2(ldsB2, 36864, 37376);
  g4_0b = __builtin_amdgcn_rcpf(g4_0b);
  SB; AM16(A1_1, w184, xf[20]);
  g4_0a = g4_0a * A0_0[0];
  g4_0b = g4_0b * A0_0[1];
  SB;
  AM16(A1_2, w185, xf[2]); SB;
  s16x8 w188 = TR2(ldsA2, 40960, 41472);
  hp0[0] = cvtpk(g4_0a, g4_0b);
  float g4_1a = A0_0[2] * A0_0[2];
  SB; AM16(A1_3, w185, xf[20]);
  float g4_1b = A0_0[3] * A0_0[3];
  g4_1a = __builtin_fmaf(g4_1a, GB, GA);
  SB;
  AM16N(A1_0, w186, xf[3], w187); SB;
  s16x8 w189 = TR2(ldsB2, 40960, 41472);
  g4_1b = __builtin_fmaf(g4_1b, GB, GA);
  g4_1a = g4_1a * A0_0[2];
  SB; AM16(A1_1, w186, xf[21]);
  g4_1b = g4_1b * A0_0[3];
  g4_1a = __builtin_amdgcn_exp2f(g4_1a);
  DMA2(lW, 99328, voffV1, srdW2, sV, 884736);
  SB;
  AM16(A1_2, w187, xf[3]); SB;
  s16x8 w190 = TR2(ldsA2, 45056, 45568);
  g4_1b = __builtin_amdgcn_exp2f(g4_1b);
  SB; AM16(A1_3, w187, xf[21]);
  g4_1a = g4_1a + 1.0f;
  g4_1b = g4_1b + 1.0f;
  SB;
  AM16N(A1_0, w188, xf[4], w189); SB;
  s16x8 w191 = TR2(ldsB2, 45056, 45568);
  g4_1a = __builtin_amdgcn_rcpf(g4_1a);
  SB; AM16(A1_1, w188, xf[22]);
  g4_1b = __builtin_amdgcn_rcpf(g4_1b);
  g4_1a = g4_1a * A0_0[2];
  SB;
  AM16(A1_2, w189, xf[4]); SB;
  s16x8 w192 = TR2(ldsA0, 0, 512);
  g4_1b = g4_1b * A0_0[3];
  hp0[1] = cvtpk(g4_1a, g4_1b);
  SB; AM16(A1_3, w189, xf[22]);
  float g4_2a = A0_2[0] * A0_2[0];
  float g4_2b = A0_2[1] * A0_2[1];
  SB;
  AM16N(A1_0, w190, xf[5], w191); SB;
  s16x8 w193 = TR2(ldsB0, 0, 512);
  g4_2a = __builtin_fmaf(g4_2a, GB, GA);
  g4_2b = __builtin_fmaf(g4_2b, GB, GA);
  SB; AM16(A1_1, w190, xf[23]);
  g4_2a = g4_2a * A0_2[0];
  g4_2b = g4_2b * A0_2[1];
  DMA2(lW, 100352, voffV2, srdW2, sV, 884736);
  SB;
  AM16(A1_2, w191, xf[5]); SB;
  s16x8 w194 = TR2(ldsA0, 4096, 4608);
  g4_2a = __builtin_amdgcn_exp2f(g4_2a);
  SB; AM16(A1_3, w191, xf[23]);
  g4_2b = __builtin_amdgcn_exp2f(g4_2b);
  g4_2a = g4_2a + 1.0f;
  SB;
  SB; asm volatile("s_waitcnt vmcnt(3)\n\ts_barrier" : "+v"(pl0) :: "memory"); SB;
  POLL2(pl0, voffHP, 2, 0);
  const u32 sH = 29360128 + ((r * 4 + j) * 36 + cg) * 8192 + rg * 2048, vhA = vl16 + cg * 1024, vhB = vl16 + 1024 - cg * 1024;
  u32x4 h0a, h0b, h1a, h1b, h2a, h2b, h3a, h3b, h4a, h4b, h5a, h5b, h6a, h6b, h7a, h7b, h8a, h8b, h9a, h9b, h10a, h10b, h11a, h11b, h12a, h12b, h13a, h13b, h14a, h14b, h15a, h15b, h16a, h16b, h17a, h17b; f32x4 Cc[24];
  FLAG(sHF, 4);
  AM16N(A1_0, w192, xf[6], w193); SB;
  s16x8 w195 = TR2(ldsB0, 4096, 4608);
  g4_2b = g4_2b + 1.0f;
  SB; AM16(A1_1, w192, xf[24]);
  g4_2a = __builtin_amdgcn_rcpf(g4_2a);
  g4_2b = __builtin_amdgcn_rcpf(g4_2b);
  SB;
  AM16(A1_2, w193, xf[6]); SB;
  s16x8 w196 = TR2(ldsA0, 8192, 8704);
  g4_2a = g4_2a * A0_2[0];
  g4_2b = g4_2b * A0_2[1];
  SB; AM16(A1_3, w193, xf[24]);
  hp0[2] = cvtpk(g4_2a, g4_2b);
  float g4_3a = A0_2[2] * A0_2[2];
  DMA2(lW, 122880, voffV0, srdW2, sV, 73728);
  SB;
  AM16N(A1_0, w194, xf[7], w195); SB;
  s16x8 w197 = TR2(ldsB0, 8192, 8704);
  float g4_3b = A0_2[3] * A0_2[3];
  g4_3a = __builtin_fmaf(g4_3a, GB, GA);
  SB; AM16(A1_1, w194, xf[25]);
  g4_3b = __builtin_fmaf(g4_3b, GB, GA);
  g4_3a = g4_3a * A0_2[2];
  g4_3b = g4_3b * A0_2[3];
  SB;
  AM16(A1_2, w195, xf[7]); SB;
  s16x8 w198 = TR2(ldsA0, 12288, 12800);
  g4_3a = __builtin_amdgcn_exp2f(g4_3a);
  SB; AM16(A1_3, w195, xf[25]);
  g4_3b = __builtin_amdgcn_exp2f(g4_3b);
  DMA2(lW, 123904, voffV1, srdW2, sV, 73728);
  SB;
  AM16N(A1_0, w196, xf[8], w197); SB;
  s16x8 w199 = TR2(ldsB0, 12288, 12800);
  g4_3a = g4_3a + 1.0f;
  SB; AM16(A1_1, w196, xf[26]);
  g4_3b = g4_3b + 1.0f;
  g4_3a = __builtin_amdgcn_rcpf(g4_3a);
  DMA2(lW, 124928, voffV2, srdW2, sV, 73728);
  SB;
  AM16(A1_2, w197, xf[8]); SB;
  s16x8 w200 = TR2(ldsA0, 16384, 16896);
  g4_3b = __builtin_amdgcn_rcpf(g4_3b);
  g4_3a = g4_3a * A0_2[2];
  SB; AM16(A1_3, w197, xf[26]);
  g4_3b = g4_3b * A0_2[3];
  hp0[3] = cvtpk(g4_3a, g4_3b);
  LOAD4(h0a, vhA, srdWS, sH + 0, 0, " sc1");
  SB;
  AM16N(A1_0, w198, xf[9], w199); SB;
  s16x8 w201 = TR2(ldsB0, 16384, 16896);
  STORE4(hp0, vl16, srdWS, sHo + rg * 2048 + 65536, 0);
  float g4_4a = A0_1[0] * A0_1[0];
  SB; AM16(A1_1, w198, xf[27]);
  float g4_4b = A0_1[1] * A0_1[1];
  g4_4a = __builtin_fmaf(g4_4a, GB, GA);
  g4_4b = __builtin_fmaf(g4_4b, GB, GA);
  SB;
  AM16(A1_2, w199, xf[9]); SB;
  s16x8 w202 = TR2(ldsA0, 20480, 20992);
  g4_4a = g4_4a * A0_1[0];
  SB; AM16(A1_3, w199, xf[27]);
  g4_4b = g4_4b * A0_1[1];
  g4_4a = __builtin_amdgcn_exp2f(g4_4a);
  LOAD4(h0b, vhB, srdWS, sH + 0, 0, " sc1");
  SB;
  AM16N(A1_0, w200, xf[10], w201); SB;
  s16x8 w203 = TR2(ldsB0, 20480, 20992);
  g4_4b = __builtin_amdgcn_exp2f(g4_4b);
  g4_4a = g4_4a + 1.0f;
  SB; AM16(A1_1, w200, xf[28]);
  g4_4b = g4_4b + 1.0f;
  g4_4a = __builtin_amdgcn_rcpf(g4_4a);
  LOAD4(h1a, vhA, srdWS, sH + 98304, 0, " sc1");
  SB;
  AM16(A1_2, w201, xf[10]); SB;
  s16x8 w204 = TR2(ldsA0, 24576, 25088);
  g4_4b = __builtin_amdgcn_rcpf(g4_4b);
  SB; AM16(A1_3, w201, xf[28]);
  g4_4a = g4_4a * A0_1[0];
  g4_4b = g4_4b * A0_1[1];
  LOAD4(h1b, vhB, srdWS, sH + 98304, 0, " sc1");
  SB;
  AM16N(A1_0, w202, xf[11], w203); SB;
  s16x8 w205 = TR2(ldsB0, 24576, 25088);
  hp1[0] = cvtpk(g4_4a, g4_4b);
  float g4_5a = A0_1[2] * A0_1[2];
  SB; AM16(A1_1, w202, xf[29]);
  float g4_5b = A0_1[3] * A0_1[3];
  g4_5a = __builtin_fmaf(g4_5a, GB, GA);
  SB;
  AM16(A1_2, w203, xf[11]); SB;
  s16x8 w206 = TR2(ldsA0, 28672, 29184);
  g4_5b = __builtin_fmaf(g4_5b, GB, GA);
  g4_5a = g4_5a * A0_1[2];
  SB; AM16(A1_3, w203, xf[29]);
  g4_5b = g4_5b * A0_1[3];
  g4_5a = __builtin_amdgcn_exp2f(g4_5a);
  LOAD4(h2a, vhA, srdWS, sH + 196608, 0, " sc1");
  SB;
  SB; asm volatile("s_barrier" :  :: "memory"); SB;
  u32x2 pl3;
  LOAD2(pl3, voffHP, srdWS, 0, 0, " sc1");
  AM16N(A1_0, w204, xf[12], w205); SB;
  s16x8 w207 = TR2(ldsB0, 28672, 29184);
  g4_5b = __builtin_amdgcn_exp2f(g4_5b);
  SB; AM16(A1_1, w204, xf[30]);
  g4_5a = g4_5a + 1.0f;
  g4_5b = g4_5b + 1.0f;
  SB;
  AM16(A1_2, w205, xf[12]); SB;
  s16x8 w208 = TR2(ldsA0, 32768, 33280);
  g4_5a = __builtin_amdgcn_rcpf(g4_5a);
  SB; AM16(A1_3, w205, xf[30]);
  g4_5b = __builtin_amdgcn_rcpf(g4_5b);
  g4_5a = g4_5a * A0_1[2];
  DMA2(lW, 0, voffV0, srdW2, sV, 516096);
  SB;
  AM16N(A1_0, w206, xf[13], w207); SB;
  s16x8 w209 = TR2(ldsB0, 32768, 33280);
  g4_5b = g4_5b * A0_1[3];
  hp1[1] = cvtpk(g4_5a, g4_5b);
  SB; AM16(A1_1, w206, xf[31]);
  float g4_6a = A0_3[0] * A0_3[0];
  float g4_6b = A0_3[1] * A0_3[1];
  const u32 g4 = ln >> 4, vbs = cg * 12288 + (VROW + q4) * 384 + (ln & 3) * 8, vx = (2 * (g4 & 1) + (q4 >> 1)) & 3;
  SB;
  AM16(A1_2, w207, xf[13]); SB;
  s16x8 w210 = TR2(ldsA0, 36864, 37376);
  g4_6a = __builtin_fmaf(g4_6a, GB, GA);
  g4_6b = __builtin_fmaf(g4_6b, GB, GA);
  SB; AM16(A1_3, w207, xf[31]);
  g4_6a = g4_6a * A0_3[0];
  g4_6b = g4_6b * A0_3[1];
  DMA2(lW, 1024, voffV1, srdW2, sV, 516096);
  SB;
  AM16N(A1_0, w208, xf[14], w209); SB;
  s16x8 w211 = TR2(ldsB0, 36864, 37376);
  g4_6a = __builtin_amdgcn_exp2f(g4_6a);
  g4_6b = __builtin_amdgcn_exp2f(g4_6b);
  SB; AM16(A1_1, w208, xf[32]);
  g4_6a = g4_6a + 1.0f;
  const u32 ldsV0_1 = OPQ(vbs + ((0 ^ vx) << 5) + 49152);
  DMA2(lW, 2048, voffV2, srdW2, sV, 516096);
  SB;
  AM16(A1_2, w209, xf[14]); SB;
  s16x8 w212 = TR2(ldsA0, 40960, 41472);
  g4_6b = g4_6b + 1.0f;
  g4_6a = __builtin_amdgcn_rcpf(g4_6a);
  SB; AM16(A1_3, w209, xf[32]);
  g4_6b = __builtin_amdgcn_rcpf(g4_6b);
  const u32 ldsV1_1 = OPQ(vbs + ((1 ^ vx) << 5) + 49152);
  LOAD4(h2b, vhB, srdWS, sH + 196608, 0, " sc1");
  SB;
  AM16N(A1_0, w210, xf[15], w211); SB;
  s16x8 w213 = TR2(ldsB0, 40960, 41472);
  g4_6a = g4_6a * A0_3[0];
  g4_6b = g4_6b * A0_3[1];
  SB; AM16(A1_1, w210, xf[33]);
  hp1[2] = cvtpk(g4_6a, g4_6b);
  float g4_7a = A0_3[2] * A0_3[2];
  const u32 ldsV2_1 = OPQ(vbs + ((2 ^ vx) << 5) + 49152);
  SB;
  AM16(A1_2, w211, xf[15]); SB;
  s16x8 w214 = TR2(ldsA0, 45056, 45568);
  float g4_7b = A0_3[3] * A0_3[3];
  g4_7a = __builtin_fmaf(g4_7a, GB, GA);
  g4_7b = __builtin_fmaf(g4_7b, GB, GA);
  SB; AM16(A1_3, w211, xf[33]);
  g4_7a = g4_7a * A0_3[2];
  g4_7b = g4_7b * A0_3[3];
  const u32 ldsV3_1 = OPQ(vbs + ((3 ^ vx) << 5) + 49152);
  LOAD4(h3a, vhA, srdWS, sH + 16384, 0, " sc1");
  SB;
  AM16N(A1_0, w212, xf[16], w213); SB;
  s16x8 w215 = TR2(ldsB0, 45056, 45568);
  g4_7a = __builtin_amdgcn_exp2f(g4_7a);
  SB; AM16(A1_1, w212, xf[34]);
  g4_7b = __builtin_amdgcn_exp2f(g4_7b);
  LOAD4(h3b, vhB, srdWS, sH + 16384, 0, " sc1");
  SB;
  AM16(A1_2, w213, xf[16]); SB;
  s16x8 w216 = TR2(ldsV0_1, 0, 6144);
  g4_7a = g4_7a + 1.0f;
  SB; AM16(A1_3, w213, xf[34]);
  g4_7b = g4_7b + 1.0f;
  g4_7a = __builtin_amdgcn_rcpf(g4_7a);
  LOAD4(h4a, vhA, srdWS, sH + 114688, 0, " sc1");
  SB;
  AM16N(A1_0, w214, xf[17], w215); SB;
  s16x8 w217 = TR2(ldsV1_1, 0, 6144);
  g4_7b = __builtin_amdgcn_rcpf(g4_7b);
  g4_7a = g4_7a * A0_3[2];
  SB; AM16(A1_1, w214, xf[35]);
  g4_7b = g4_7b * A0_3[3];
  hp1[3] = cvtpk(g4_7a, g4_7b);
  SB;
  AM16(A1_2, w215, xf[17]); SB;
  s16x8 w218 = TR2(ldsV2_1, 0, 6144);
  SB; AM16(A1_3, w215, xf[35]);
  STORE4(hp1, vl16, srdWS, sHo + rg * 2048 + 65536, 1024);
  LOAD4(h4b, vhB, srdWS, sH + 114688, 0, " sc1");
  SB;
  SB; asm volatile("s_waitcnt vmcnt(13)\n\ts_barrier" : "+v"(h0a), "+v"(h0b) :: "memory"); SB;
  AM16Z(Cc[0], w216, h0a);
  SB;
  s16x8 w219 = TR2(ldsV3_1, 0, 6144);
  SB;
  AM16Z(Cc[12], w216, h0b);
  SB;
  AM16Z(Cc[1], w217, h0a);
  SB;
  s16x8 w220 = TR2(ldsV0_1, 128, 6272);
  SB;
  AM16Z(Cc[13], w217, h0b);
  float g5_0a = A1_0[0] * A1_0[0];
  float g5_0b = A1_0[1] * A1_0[1];
  g5_0a = __builtin_fmaf(g5_0a, GB, GA);
  SB;
  AM16Z(Cc[2], w218, h0a);
  SB;
  s16x8 w221 = TR2(ldsV1_1, 128, 6272);
  SB;
  AM16Z(Cc[14], w218, h0b);
  g5_0b = __builtin_fmaf(g5_0b, GB, GA);
  g5_0a = g5_0a * A1_0[0];
  LOAD4(h5a, vhA, srdWS, sH + 212992, 0, " sc1");
  SB;
  AM16Z(Cc[3], w219, h0a);
  SB;
  s16x8 w222 = TR2(ldsV2_1, 128, 6272);
  SB;
  AM16Z(Cc[15], w219, h0b);
  g5_0b = g5_0b * A1_0[1];
  g5_0a = __builtin_amdgcn_exp2f(g5_0a);
  SB;
  AM16Z(Cc[4], w220, h0a);
  SB;
  s16x8 w223 = TR2(ldsV3_1, 128, 6272);
  SB;
  AM16Z(Cc[16], w220, h0b);
  g5_0b = __builtin_amdgcn_exp2f(g5_0b);
  g5_0a = g5_0a + 1.0f;
  LOAD4(h5b, vhB, srdWS, sH + 212992, 0, " sc1");
  SB;
  AM16Z(Cc[5], w221, h0a);
  SB;
  s16x8 w224 = TR2(ldsV0_1, 256, 6400);
  SB;
  AM16Z(Cc[17], w221, h0b);
  g5_0b = g5_0b + 1.0f;
  g5_0a = __builtin_amdgcn_rcpf(g5_0a);
  SB;
  AM16Z(Cc[6], w222, h0a);
  SB;
  s16x8 w225 = TR2(ldsV1_1, 256, 6400);
  SB;
  AM16Z(Cc[18], w222, h0b);
  g5_0b = __builtin_amdgcn_rcpf(g5_0b);
  DMA2(lW, 24576, voffV0, srdW2, sV, 958464);
  SB;
  AM16Z(Cc[7], w223, h0a);
  SB;
  s16x8 w226 = TR2(ldsV2_1, 256, 6400);
  SB;
  AM16Z(Cc[19], w223, h0b);
  g5_0a = g5_0a * A1_0[0];
  g5_0b = g5_0b * A1_0[1];
  SB;
  AM16Z(Cc[8], w224, h0a);
  SB;
  s16x8 w227 = TR2(ldsV3_1, 256, 6400);
  SB;
  AM16Z(Cc[20], w224, h0b);
  hp0[0] = cvtpk(g5_0a, g5_0b);
  float g5_1a = A1_0[2] * A1_0[2];
  float g5_1b = A1_0[3] * A1_0[3];
  DMA2(lW, 25600, voffV1, srdW2, sV, 958464);
  SB;
  AM16Z(Cc[9], w225, h0a);
  SB;
  s16x8 w228 = TR2(ldsV0_1, 24576, 30720);
  SB;
  AM16Z(Cc[21], w225, h0b);
  g5_1a = __builtin_fmaf(g5_1a, GB, GA);
  g5_1b = __builtin_fmaf(g5_1b, GB, GA);
  g5_1a = g5_1a * A1_0[2];
  SB;
  AM16Z(Cc[10], w226, h0a);
  SB;
  s16x8 w229 = TR2(ldsV1_1, 24576, 30720);
  SB;
  AM16Z(Cc[22], w226, h0b);
  g5_1b = g5_1b * A1_0[3];
  g5_1a = __builtin_amdgcn_exp2f(g5_1a);
  DMA2(lW, 26624, voffV2, srdW2, sV, 958464);
  SB;
  AM16Z(Cc[11], w227, h0a);
  SB;
  s16x8 w230 = TR2(ldsV2_1, 24576, 30720);
  SB;
  AM16Z(Cc[23], w227, h0b);
  g5_1b = __builtin_amdgcn_exp2f(g5_1b);
  SB;
  SB; asm volatile("s_waitcnt vmcnt(6)\n\ts_barrier" : "+v"(pl3), "+v"(h1a), "+v"(h1b) :: "memory"); SB;
  POLL2(pl3, voffHP, 3, 3);
  FLAG(sHF, 5);
  AM16N(Cc[0], w228, h1a, w229);
  SB;
  s16x8 w231 = TR2(ldsV3_1, 24576, 30720);
  SB;
  AM16(Cc[12], w228, h1b);
  g5_1a = g5_1a + 1.0f;
  g5_1b = g5_1b + 1.0f;
  SB;
  AM16(Cc[1], w229, h1a);
  SB;
  s16x8 w232 = TR2(ldsV0_1, 24704, 30848);
  SB;
  AM16(Cc[13], w229, h1b);
  g5_1a = __builtin_amdgcn_rcpf(g5_1a);
  g5_1b = __builtin_amdgcn_rcpf(g5_1b);
  const u32 ldsV0_2 = OPQ(vbs + ((0 ^ vx) << 5) + 98304);
  SB;
  AM16N(Cc[2], w230, h1a, w231);
  SB;
  s16x8 w233 = TR2(ldsV1_1, 24704, 30848);
  SB;
  AM16(Cc[14], w230, h1b);
  g5_1a = g5_1a * A1_0[2];
  g5_1b = g5_1b * A1_0[3];
  LOAD4(h6a, vhA, srdWS, sH + 32768, 0, " sc1");
  SB;
  AM16(Cc[3], w231, h1a);
  SB;
  s16x8 w234 = TR2(ldsV2_1, 24704, 30848);
  SB;
  AM16(Cc[15], w231, h1b);
  hp0[1] = cvtpk(g5_1a, g5_1b);
  float g5_2a = A1_2[0] * A1_2[0];
  float g5_2b = A1_2[1] * A1_2[1];
  const u32 ldsV1_2 = OPQ(vbs + ((1 ^ vx) << 5) + 98304);
  SB;
  AM16N(Cc[4], w232, h1a, w233);
  SB;
  s16x8 w235 = TR2(ldsV3_1, 24704, 30848);
  SB;
  AM16(Cc[16], w232, h1b);
  g5_2a = __builtin_fmaf(g5_2a, GB, GA);
  g5_2b = __builtin_fmaf(g5_2b, GB, GA);
  LOAD4(h6b, vhB, srdWS, sH + 32768, 0, " sc1");
  SB;
  AM16(Cc[5], w233, h1a);
  SB;
  s16x8 w236 = TR2(ldsV0_1, 24832, 30976);
  SB;
  AM16(Cc[17], w233, h1b);
  g5_2a = g5_2a * A1_2[0];
  g5_2b = g5_2b * A1_2[1];
  const u32 ldsV2_2 = OPQ(vbs + ((2 ^ vx) << 5) + 98304);
  SB;
  AM16N(Cc[6], w234, h1a, w235);
  SB;
  s16x8 w237 = TR2(ldsV1_1, 24832, 30976);
  SB;
  AM16(Cc[18], w234, h1b);
  g5_2a = __builtin_amdgcn_exp2f(g5_2a);
  g5_2b = __builtin_amdgcn_exp2f(g5_2b);
  DMA2(lW, 49152, voffV0, srdW2, sV, 147456);
  SB;
  AM16(Cc[7], w235, h1a);
  SB;
  s16x8 w238 = TR2(ldsV2_1, 24832, 30976);
  SB;
  AM16(Cc[19], w235, h1b);
  g5_2a = g5_2a + 1.0f;
  g5_2b = g5_2b + 1.0f;
  const u32 ldsV3_2 = OPQ(vbs + ((3 ^ vx) << 5) + 98304);
  SB;
  AM16N(Cc[8], w236, h1a, w237);
  SB;
  s16x8 w239 = TR2(ldsV3_1, 24832, 30976);
  SB;
  AM16(Cc[20], w236, h1b);
  g5_2a = __builtin_amdgcn_rcpf(g5_2a);
  DMA2(lW, 50176, voffV1, srdW2, sV, 147456);
  SB;
  AM16(Cc[9], w237, h1a);
  SB;
  s16x8 w240 = TR2(ldsV0_2, 0, 6144);
  SB;
  AM16(Cc[21], w237, h1b);
  g5_2b = __builtin_amdgcn_rcpf(g5_2b);
  g5_2a = g5_2a * A1_2[0];
  SB;
  AM16N(Cc[10], w238, h1a, w239);
  SB;
  s16x8 w241 = TR2(ldsV1_2, 0, 6144);
  SB;
  AM16(Cc[22], w238, h1b);
  g5_2b = g5_2b * A1_2[1];
  hp0[2] = cvtpk(g5_2a, g5_2b);
  float g5_3a = A1_2[2] * A1_2[2];
  DMA2(lW, 51200, voffV2, srdW2, sV, 147456);
  SB;
  AM16(Cc[11], w239, h1a);
  SB;
  s16x8 w242 = TR2(ldsV2_2, 0, 6144);
  SB;
  AM16(Cc[23], w239, h1b);
  float g5_3b = A1_2[3] * A1_2[3];
  g5_3a = __builtin_fmaf(g5_3a, GB, GA);
  g5_3b = __builtin_fmaf(g5_3b, GB, GA);
  SB;
  SB; asm volatile("s_barrier" : "+v"(h2a), "+v"(h2b) :: "memory"); SB;
  u32x2 pl4;
  LOAD2(pl4, voffHP, srdWS, 0, 0, " sc1");
  AM16N(Cc[0], w240, h2a, w241);
  SB;
  s16x8 w243 = TR2(ldsV3_2, 0, 6144);
  SB;
  AM16(Cc[12], w240, h2b);
  g5_3a = g5_3a * A1_2[2];
  g5_3b = g5_3b * A1_2[3];
  SB;
  AM16(Cc[1], w241, h2a);
  SB;
  s16x8 w244 = TR2(ldsV0_2, 128, 6272);
  SB;
  AM16(Cc[13], w241, h2b);
  g5_3a = __builtin_amdgcn_exp2f(g5_3a);
  SB;
  AM16N(Cc[2], w242, h2a, w243);
  SB;
  s16x8 w245 = TR2(ldsV1_2, 128, 6272);
  SB;
  AM16(Cc[14], w242, h2b);
  g5_3b = __builtin_amdgcn_exp2f(g5_3b);
  g5_3a = g5_3a + 1.0f;
  g5_3b = g5_3b + 1.0f;
  LOAD4(h7a, vhA, srdWS, sH + 131072, 0, " sc1");
  SB;
  AM16(Cc[3], w243, h2a);
  SB;
  s16x8 w246 = TR2(ldsV2_2, 128, 6272);
  SB;
  AM16(Cc[15], w243, h2b);
  g5_3a = __builtin_amdgcn_rcpf(g5_3a);
  SB;
  AM16N(Cc[4], w244, h2a, w245);
  SB;
  s16x8 w247 = TR2(ldsV3_2, 128, 6272);
  SB;
  AM16(Cc[16], w244, h2b);
  g5_3b = __builtin_amdgcn_rcpf(g5_3b);
  g5_3a = g5_3a * A1_2[2];
  LOAD4(h7b, vhB, srdWS, sH + 131072, 0, " sc1");
  SB;
  AM16(Cc[5], w245, h2a);
  SB;
  s16x8 w248 = TR2(ldsV0_2, 256, 6400);
  SB;
  AM16(Cc[17], w245, h2b);
  g5_3b = g5_3b * A1_2[3];
  hp0[3] = cvtpk(g5_3a, g5_3b);
  SB;
  AM16N(Cc[6], w246, h2a, w247);
  SB;
  s16x8 w249 = TR2(ldsV1_2, 256, 6400);
  SB;
  AM16(Cc[18], w246, h2b);
  STORE4(hp0, vl16, srdWS, sHo + rg * 2048 + 81920, 0);
  float g5_4a = A1_1[0] * A1_1[0];
  float g5_4b = A1_1[1] * A1_1[1];
  g5_4a = __builtin_fmaf(g5_4a, GB, GA);
  DMA2(lW, 73728, voffV0, srdW2, sV, 589824);
  SB;
  AM16(Cc[7], w247, h2a);
  SB;
  s16x8 w250 = TR2(ldsV2_2, 256, 6400);
  SB;
  AM16(Cc[19], w247, h2b);
  g5_4b = __builtin_fmaf(g5_4b, GB, GA);
  g5_4a = g5_4a * A1_1[0];
  SB;
  AM16N(Cc[8], w248, h2a, w249);
  SB;
  s16x8 w251 = TR2(ldsV3_2, 256, 6400);
  SB;
  AM16(Cc[20], w248, h2b);
  g5_4b = g5_4b * A1_1[1];
  g5_4a = __builtin_amdgcn_exp2f(g5_4a);
  DMA2(lW, 74752, voffV1, srdW2, sV, 589824);
  SB;
  AM16(Cc[9], w249, h2a);
  SB;
  s16x8 w252 = TR2(ldsV0_2, 24576, 30720);
  SB;
  AM16(Cc[21], w249, h2b);
  g5_4b = __builtin_amdgcn_exp2f(g5_4b);
  g5_4a = g5_4a + 1.0f;
  SB;
  AM16N(Cc[10], w250, h2a, w251);
  SB;
  s16x8 w253 = TR2(ldsV1_2, 24576, 30720);
  SB;
  AM16(Cc[22], w250, h2b);
  g5_4b = g5_4b + 1.0f;
  g5_4a = __builtin_amdgcn_rcpf(g5_4a);
  DMA2(lW, 75776, voffV2, srdW2, sV, 589824);
  SB;
  AM16(Cc[11], w251, h2a);
  SB;
  s16x8 w254 = TR2(ldsV2_2, 24576, 30720);
  SB;
  AM16(Cc[23], w251, h2b);
  g5_4b = __builtin_amdgcn_rcpf(g5_4b);
  SB;
  SB; asm volatile("s_barrier" : "+v"(h3a), "+v"(h3b) :: "memory"); SB;
  AM16N(Cc[0], w252, h3a, w253);
  SB;
  s16x8 w255 = TR2(ldsV3_2, 24576, 30720);
  SB;
  AM16(Cc[12], w252, h3b);
  g5_4a = g5_4a * A1_1[0];
  g5_4b = g5_4b * A1_1[1];
  SB;
  AM16(Cc[1], w253, h3a);
  SB;
  s16x8 w256 = TR2(ldsV0_2, 24704, 30848);
  SB;
  AM16(Cc[13], w253, h3b);
  hp1[0] = cvtpk(g5_4a, g5_4b);
  float g5_5a = A1_1[2] * A1_1[2];
  float g5_5b = A1_1[3] * A1_1[3];
  const u32 ldsV0_0 = OPQ(vbs + ((0 ^ vx) << 5) + 0);
  SB;
  AM16N(Cc[2], w254, h3a, w255);
  SB;
  s16x8 w257 = TR2(ldsV1_2, 24704, 30848);
  SB;
  AM16(Cc[14], w254, h3b);
  g5_5a = __builtin_fmaf(g5_5a, GB, GA);
  g5_5b = __builtin_fmaf(g5_5b, GB, GA);
  g5_5a = g5_5a * A1_1[2];
  LOAD4(h8a, vhA, srdWS, sH + 229376, 0, " sc1");
  SB;
  AM16(Cc[3], w255, h3a);
  SB;
  s16x8 w258 = TR2(ldsV2_2, 24704, 30848);
  SB;
  AM16(Cc[15], w255, h3b);
  g5_5b = g5_5b * A1_1[3];
  g5_5a = __builtin_amdgcn_exp2f(g5_5a);
  const u32 ldsV1_0 = OPQ(vbs + ((1 ^ vx) << 5) + 0);
  SB;
  AM16N(Cc[4], w256, h3a, w257);
  SB;
  s16x8 w259 = TR2(ldsV3_2, 24704, 30848);
  SB;
  AM16(Cc[16], w256, h3b);
  g5_5b = __builtin_amdgcn_exp2f(g5_5b);
  LOAD4(h8b, vhB, srdWS, sH + 229376, 0, " sc1");
  SB;
  AM16(Cc[5], w257, h3a);
  SB;
  s16x8 w260 = TR2(ldsV0_2, 24832, 30976);
  SB;
  AM16(Cc[17], w257, h3b);
  g5_5a = g5_5a + 1.0f;
  g5_5b = g5_5b + 1.0f;
  const u32 ldsV2_0 = OPQ(vbs + ((2 ^ vx) << 5) + 0);
  SB;
  AM16N(Cc[6], w258, h3a, w259);
  SB;
  s16x8 w261 = TR2(ldsV1_2, 24832, 30976);
  SB;
  AM16(Cc[18], w258, h3b);
  g5_5a = __builtin_amdgcn_rcpf(g5_5a);
  g5_5b = __builtin_amdgcn_rcpf(g5_5b);
  DMA2(lW, 98304, voffV0, srdW2, sV, 1032192);
  SB;
  AM16(Cc[7], w259, h3a);
  SB;
  s16x8 w262 = TR2(ldsV2_2, 24832, 30976);
  SB;
  AM16(Cc[19], w259, h3b);
  g5_5a = g5_5a * A1_1[2];
  g5_5b = g5_5b * A1_1[3];
  const u32 ldsV3_0 = OPQ(vbs + ((3 ^ vx) << 5) + 0);
  SB;
  AM16N(Cc[8], w260, h3a, w261);
  SB;
  s16x8 w263 = TR2(ldsV3_2, 24832, 30976);
  SB;
  AM16(Cc[20], w260, h3b);
  hp1[1] = cvtpk(g5_5a, g5_5b);
  float g5_6a = A1_3[0] * A1_3[0];
  float g5_6b = A1_3[1] * A1_3[1];
  DMA2(lW, 99328, voffV1, srdW2, sV, 1032192);
  SB;
  AM16(Cc[9], w261, h3a);
  SB;
  s16x8 w264 = TR2(ldsV0_0, 0, 6144);
  SB;
  AM16(Cc[21], w261, h3b);
  g5_6a = __builtin_fmaf(g5_6a, GB, GA);
  g5_6b = __builtin_fmaf(g5_6b, GB, GA);
  SB;
  AM16N(Cc[10], w262, h3a, w263);
  SB;
  s16x8 w265 = TR2(ldsV1_0, 0, 6144);
  SB;
  AM16(Cc[22], w262, h3b);
  g5_6a = g5_6a * A1_3[0];
  g5_6b = g5_6b * A1_3[1];
  DMA2(lW, 100352, voffV2, srdW2, sV, 1032192);
  SB;
  AM16(Cc[11], w263, h3a);
  SB;
  s16x8 w266 = TR2(ldsV2_0, 0, 6144);
  SB;
  AM16(Cc[23], w263, h3b);
  g5_6a = __builtin_amdgcn_exp2f(g5_6a);
  g5_6b = __builtin_amdgcn_exp2f(g5_6b);
  SB;
  SB; asm volatile("s_waitcnt vmcnt(11)\n\ts_barrier" : "+v"(pl4), "+v"(h4a), "+v"(h4b) :: "memory"); SB;
  POLL2(pl4, voffHP, 4, 4);
  AM16N(Cc[0], w264, h4a, w265);
  SB;
  s16x8 w267 = TR2(ldsV3_0, 0, 6144);
  SB;
  AM16(Cc[12], w264, h4b);
  g5_6a = g5_6a + 1.0f;
  g5_6b = g5_6b + 1.0f;
  SB;
  AM16(Cc[1], w265, h4a);
  SB;
  s16x8 w268 = TR2(ldsV0_0, 128, 6272);
  SB;
  AM16(Cc[13], w265, h4b);
  g5_6a = __builtin_amdgcn_rcpf(g5_6a);
  SB;
  AM16N(Cc[2], w266, h4a, w267);
  SB;
  s16x8 w269 = TR2(ldsV1_0, 128, 6272);
  SB;
  AM16(Cc[14], w266, h4b);
  g5_6b = __builtin_amdgcn_rcpf(g5_6b);
  g5_6a = g5_6a * A1_3[0];
  LOAD4(h9a, vhA, srdWS, sH + 49152, 0, " sc1");
  SB;
  AM16(Cc[3], w267, h4a);
  SB;
  s16x8 w270 = TR2(ldsV2_0, 128, 6272);
  SB;
  AM16(Cc[15], w267, h4b);
  g5_6b = g5_6b * A1_3[1];
  hp1[2] = cvtpk(g5_6a, g5_6b);
  float g5_7a = A1_3[2] * A1_3[2];
  SB;
  AM16N(Cc[4], w268, h4a, w269);
  SB;
  s16x8 w271 = TR2(ldsV3_0, 128, 6272);
  SB;
  AM16(Cc[16], w268, h4b);
  float g5_7b = A1_3[3] * A1_3[3];
  g5_7a = __builtin_fmaf(g5_7a, GB, GA);
  g5_7b = __builtin_fmaf(g5_7b, GB, GA);
  LOAD4(h9b, vhB, srdWS, sH + 49152, 0, " sc1");
  SB;
  AM16(Cc[5], w269, h4a);
  SB;
  s16x8 w272 = TR2(ldsV0_0, 256, 6400);
  SB;
  AM16(Cc[17], w269, h4b);
  g5_7a = g5_7a * A1_3[2];
  g5_7b = g5_7b * A1_3[3];
  SB;
  AM16N(Cc[6], w270, h4a, w271);
  SB;
  s16x8 w273 = TR2(ldsV1_0, 256, 6400);
  SB;
  AM16(Cc[18], w270, h4b);
  g5_7a = __builtin_amdgcn_exp2f(g5_7a);
  DMA2(lW, 122880, voffV0, srdW2, sV, 221184);
  SB;
  AM16(Cc[7], w271, h4a);
  SB;
  s16x8 w274 = TR2(ldsV2_0, 256, 6400);
  SB;
  AM16(Cc[19], w271, h4b);
  g5_7b = __builtin_amdgcn_exp2f(g5_7b);
  g5_7a = g5_7a + 1.0f;
  g5_7b = g5_7b + 1.0f;
  SB;
  AM16N(Cc[8], w272, h4a, w273);
  SB;
  s16x8 w275 = TR2(ldsV3_0, 256, 6400);
  SB;
  AM16(Cc[20], w272, h4b);
  g5_7a = __builtin_amdgcn_rcpf(g5_7a);
  DMA2(lW, 123904, voffV1, srdW2, sV, 221184);
  SB;
  AM16(Cc[9], w273, h4a);
  SB;
  s16x8 w276 = TR2(ldsV0_0, 24576, 30720);
  SB;
  AM16(Cc[21], w273, h4b);
  g5_7b = __builtin_amdgcn_rcpf(g5_7b);
  g5_7a = g5_7a * A1_3[2];
  SB;
  AM16N(Cc[10], w274, h4a, w275);
  SB;
  s16x8 w277 = TR2(ldsV1_0, 24576, 30720);
  SB;
  AM16(Cc[22], w274, h4b);
  g5_7b = g5_7b * A1_3[3];
  hp1[3] = cvtpk(g5_7a, g5_7b);
  DMA2(lW, 124928, voffV2, srdW2, sV, 221184);
  SB;
  AM16(Cc[11], w275, h4a);
  SB;
  s16x8 w278 = TR2(ldsV2_0, 24576, 30720);
  SB;
  AM16(Cc[23], w275, h4b);
  STORE4(hp1, vl16, srdWS, sHo + rg * 2048 + 81920, 1024);
  SB;
  SB; asm volatile("s_barrier" : "+v"(h5a), "+v"(h5b) :: "memory"); SB;
  u32x2 pl5;
  LOAD2(pl5, voffHP, srdWS, 0, 0, " sc1");
  AM16N(Cc[0], w276, h5a, w277);
  SB;
  s16x8 w279 = TR2(ldsV3_0, 24576, 30720);
  SB;
  AM16(Cc[12], w276, h5b);
  SB;
  AM16(Cc[1], w277, h5a);
  SB;
  s16x8 w280 = TR2(ldsV0_0, 24704, 30848);
  SB;
  AM16(Cc[13], w277, h5b);
  SB;
  AM16N(Cc[2], w278, h5a, w279);
  SB;
  s16x8 w281 = TR2(ldsV1_0, 24704, 30848);
  SB;
  AM16(Cc[14], w278, h5b);
  LOAD4(h10a, vhA, srdWS, sH + 147456, 0, " sc1");
  SB;
  AM16(Cc[3], w279, h5a);
  SB;
  s16x8 w282 = TR2(ldsV2_0, 24704, 30848);
  SB;
  AM16(Cc[15], w279, h5b);
  SB;
  AM16N(Cc[4], w280, h5a, w281);
  SB;
  s16x8 w283 = TR2(ldsV3_0, 24704, 30848);
  SB;
  AM16(Cc[16], w280, h5b);
  LOAD4(h10b, vhB, srdWS, sH + 147456, 0, " sc1");
  SB;
  AM16(Cc[5], w281, h5a);
  SB;
  s16x8 w284 = TR2(ldsV0_0, 24832, 30976);
  SB;
  AM16(Cc[17], w281, h5b);
  SB;
  AM16N(Cc[6], w282, h5a, w283);
  SB;
  s16x8 w285 = TR2(ldsV1_0, 24832, 30976);
  SB;
  AM16(Cc[18], w282, h5b);
  DMA2(lW, 0, voffV0, srdW2, sV, 663552);
  SB;
  AM16(Cc[7], w283, h5a);
  SB;
  s16x8 w286 = TR2(ldsV2_0, 24832, 30976);
  SB;
  AM16(Cc[19], w283, h5b);
  SB;
  AM16N(Cc[8], w284, h5a, w285);
  SB;
  s16x8 w287 = TR2(ldsV3_0, 24832, 30976);
  SB;
  AM16(Cc[20], w284, h5b);
  DMA2(lW, 1024, voffV1, srdW2, sV, 663552);
  SB;
  AM16(Cc[9], w285, h5a);
  SB;
  s16x8 w288 = TR2(ldsV0_1, 0, 6144);
  SB;
  AM16(Cc[21], w285, h5b);
  SB;
  AM16N(Cc[10], w286, h5a, w287);
  SB;
  s16x8 w289 = TR2(ldsV1_1, 0, 6144);
  SB;
  AM16(Cc[22], w286, h5b);
  DMA2(lW, 2048, voffV2, srdW2, sV, 663552);
  SB;
  AM16(Cc[11], w287, h5a);
  SB;
  s16x8 w290 = TR2(ldsV2_1, 0, 6144);
  SB;
  AM16(Cc[23], w287, h5b);
  SB;
  SB; asm volatile("s_waitcnt vmcnt(6)\n\ts_barrier" : "+v"(h6a), "+v"(h6b) :: "memory"); SB;
  FLAG(sHF, 6);
  AM16N(Cc[0], w288, h6a, w289);
  SB;
  s16x8 w291 = TR2(ldsV3_1, 0, 6144);
  SB;
  AM16(Cc[12], w288, h6b);
  SB;
  AM16(Cc[1], w289, h6a);
  SB;
  s16x8 w292 = TR2(ldsV0_1, 128, 6272);
  SB;
  AM16(Cc[13], w289, h6b);
  SB;
  AM16N(Cc[2], w290, h6a, w291);
  SB;
  s16x8 w293 = TR2(ldsV1_1, 128, 6272);
  SB;
  AM16(Cc[14], w290, h6b);
  LOAD4(h11a, vhA, srdWS, sH + 245760, 0, " sc1");
  SB;
  AM16(Cc[3], w291, h6a);
  SB;
  s16x8 w294 = TR2(ldsV2_1, 128, 6272);
  SB;
  AM16(Cc[15], w291, h6b);
  SB;
  AM16N(Cc[4], w292, h6a, w293);
  SB;
  s16x8 w295 = TR2(ldsV3_1, 128, 6272);
  SB;
  AM16(Cc[16], w292, h6b);
  LOAD4(h11b, vhB, srdWS, sH + 245760, 0, " sc1");
  SB;
  AM16(Cc[5], w293, h6a);
  SB;
  s16x8 w296 = TR2(ldsV0_1, 256, 6400);
  SB;
  AM16(Cc[17], w293, h6b);
  SB;
  AM16N(Cc[6], w294, h6a, w295);
  SB;
  s16x8 w297 = TR2(ldsV1_1, 256, 6400);
  SB;
  AM16(Cc[18], w294, h6b);
  DMA2(lW, 24576, voffV0, srdW2, sV, 1105920);
  SB;
  AM16(Cc[7], w295, h6a);
  SB;
  s16x8 w298 = TR2(ldsV2_1, 256, 6400);
  SB;
  AM16(Cc[19], w295, h6b);
  SB;
  AM16N(Cc[8], w296, h6a, w297);
  SB;
  s16x8 w299 = TR2(ldsV3_1, 256, 6400);
  SB;
  AM16(Cc[20], w296, h6b);
  DMA2(lW, 25600, voffV1, srdW2, sV, 1105920);
  SB;
  AM16(Cc[9], w297, h6a);
  SB;
  s16x8 w300 = TR2(ldsV0_1, 24576, 30720);
  SB;
  AM16(Cc[21], w297, h6b);
  SB;
  AM16N(Cc[10], w298, h6a, w299);
  SB;
  s16x8 w301 = TR2(ldsV1_1, 24576, 30720);
  SB;
  AM16(Cc[22], w298, h6b);
  DMA2(lW, 26624, voffV2, srdW2, sV, 1105920);
  SB;
  AM16(Cc[11], w299, h6a);
  SB;
  s16x8 w302 = TR2(ldsV2_1, 24576, 30720);
  SB;
  AM16(Cc[23], w299, h6b);
  SB;
  SB; asm volatile("s_waitcnt vmcnt(11)\n\ts_barrier" : "+v"(pl5), "+v"(h7a), "+v"(h7b) :: "memory"); SB;
  POLL2(pl5, voffHP, 5, 5);
  AM16N(Cc[0], w300, h7a, w301);
  SB;
  s16x8 w303 = TR2(ldsV3_1, 24576, 30720);
  SB;
  AM16(Cc[12], w300, h7b);
  SB;
  AM16(Cc[1], w301, h7a);
  SB;
  s16x8 w304 = TR2(ldsV0_1, 24704, 30848);
  SB;
  AM16(Cc[13], w301, h7b);
  SB;
  AM16N(Cc[2], w302, h7a, w303);
  SB;
  s16x8 w305 = TR2(ldsV1_1, 24704, 30848);
  SB;
  AM16(Cc[14], w302, h7b);
  LOAD4(h12a, vhA, srdWS, sH + 65536, 0, " sc1");
  SB;
  AM16(Cc[3], w303, h7a);
  SB;
  s16x8 w306 = TR2(ldsV2_1, 24704, 30848);
  SB;
  AM16(Cc[15], w303, h7b);
  SB;
  AM16N(Cc[4], w304, h7a, w305);
  SB;
  s16x8 w307 = TR2(ldsV3_1, 24704, 30848);
  SB;
  AM16(Cc[16], w304, h7b);
  LOAD4(h12b, vhB, srdWS, sH + 65536, 0, " sc1");
  SB;
  AM16(Cc[5], w305, h7a);
  SB;
  s16x8 w308 = TR2(ldsV0_1, 24832, 30976);
  SB;
  AM16(Cc[17], w305, h7b);
  SB;
  AM16N(Cc[6], w306, h7a, w307);
  SB;
  s16x8 w309 = TR2(ldsV1_1, 24832, 30976);
  SB;
  AM16(Cc[18], w306, h7b);
  DMA2(lW, 49152, voffV0, srdW2, sV, 294912);
  SB;
  AM16(Cc[7], w307, h7a);
  SB;
  s16x8 w310 = TR2(ldsV2_1, 24832, 30976);
  SB;
  AM16(Cc[19], w307, h7b);
  SB;
  AM16N(Cc[8], w308, h7a, w309);
  SB;
  s16x8 w311 = TR2(ldsV3_1, 24832, 30976);
  SB;
  AM16(Cc[20], w308, h7b);
  DMA2(lW, 50176, voffV1, srdW2, sV, 294912);
  SB;
  AM16(Cc[9], w309, h7a);
  SB;
  s16x8 w312 = TR2(ldsV0_2, 0, 6144);
  SB;
  AM16(Cc[21], w309, h7b);
  SB;
  AM16N(Cc[10], w310, h7a, w311);
  SB;
  s16x8 w313 = TR2(ldsV1_2, 0, 6144);
  SB;
  AM16(Cc[22], w310, h7b);
  DMA2(lW, 51200, voffV2, srdW2, sV, 294912);
  SB;
  AM16(Cc[11], w311, h7a);
  SB;
  s16x8 w314 = TR2(ldsV2_2, 0, 6144);
  SB;
  AM16(Cc[23], w311, h7b);
  SB;
  SB; asm volatile("s_barrier" : "+v"(h8a), "+v"(h8b) :: "memory"); SB;
  u32x2 pl6;
  LOAD2(pl6, voffHP, srdWS, 0, 0, " sc1");
  AM16N(Cc[0], w312, h8a, w313);
  SB;
  s16x8 w315 = TR2(ldsV3_2, 0, 6144);
  SB;
  AM16(Cc[12], w312, h8b);
  SB;
  AM16(Cc[1], w313, h8a);
  SB;
  s16x8 w316 = TR2(ldsV0_2, 128, 6272);
  SB;
  AM16(Cc[13], w313, h8b);
  SB;
  AM16N(Cc[2], w314, h8a, w315);
  SB;
  s16x8 w317 = TR2(ldsV1_2, 128, 6272);
  SB;
  AM16(Cc[14], w314, h8b);
  LOAD4(h13a, vhA, srdWS, sH + 163840, 0, " sc1");
  SB;
  AM16(Cc[3], w315, h8a);
  SB;
  s16x8 w318 = TR2(ldsV2_2, 128, 6272);
  SB;
  AM16(Cc[15], w315, h8b);
  SB;
  AM16N(Cc[4], w316, h8a, w317);
  SB;
  s16x8 w319 = TR2(ldsV3_2, 128, 6272);
  SB;
  AM16(Cc[16], w316, h8b);
  LOAD4(h13b, vhB, srdWS, sH + 163840, 0, " sc1");
  SB;
  AM16(Cc[5], w317, h8a);
  SB;
  s16x8 w320 = TR2(ldsV0_2, 256, 6400);
  SB;
  AM16(Cc[17], w317, h8b);
  SB;
  AM16N(Cc[6], w318, h8a, w319);
  SB;
  s16x8 w321 = TR2(ldsV1_2, 256, 6400);
  SB;
  AM16(Cc[18], w318, h8b);
  DMA2(lW, 73728, voffV0, srdW2, sV, 737280);
  SB;
  AM16(Cc[7], w319, h8a);
  SB;
  s16x8 w322 = TR2(ldsV2_2, 256, 6400);
  SB;
  AM16(Cc[19], w319, h8b);
  SB;
  AM16N(Cc[8], w320, h8a, w321);
  SB;
  s16x8 w323 = TR2(ldsV3_2, 256, 6400);
  SB;
  AM16(Cc[20], w320, h8b);
  DMA2(lW, 74752, voffV1, srdW2, sV, 737280);
  SB;
  AM16(Cc[9], w321, h8a);
  SB;
  s16x8 w324 = TR2(ldsV0_2, 24576, 30720);
  SB;
  AM16(Cc[21], w321, h8b);
  SB;
  AM16N(Cc[10], w322, h8a, w323);
  SB;
  s16x8 w325 = TR2(ldsV1_2, 24576, 30720);
  SB;
  AM16(Cc[22], w322, h8b);
  DMA2(lW, 75776, voffV2, srdW2, sV, 737280);
  SB;
  AM16(Cc[11], w323, h8a);
  SB;
  s16x8 w326 = TR2(ldsV2_2, 24576, 30720);
  SB;
  AM16(Cc[23], w323, h8b);
  SB;
  SB; asm volatile("s_waitcnt vmcnt(17)\n\ts_barrier" : "+v"(h9a), "+v"(h9b) :: "memory"); SB;
  AM16N(Cc[0], w324, h9a, w325);
  SB;
  s16x8 w327 = TR2(ldsV3_2, 24576, 30720);
  SB;
  AM16(Cc[12], w324, h9b);
  SB;
  AM16(Cc[1], w325, h9a);
  SB;
  s16x8 w328 = TR2(ldsV0_2, 24704, 30848);
  SB;
  AM16(Cc[13], w325, h9b);
  SB;
  AM16N(Cc[2], w326, h9a, w327);
  SB;
  s16x8 w329 = TR2(ldsV1_2, 24704, 30848);
  SB;
  AM16(Cc[14], w326, h9b);
  LOAD4(h14a, vhA, srdWS, sH + 262144, 0, " sc1");
  SB;
  AM16(Cc[3], w327, h9a);
  SB;
  s16x8 w330 = TR2(ldsV2_2, 24704, 30848);
  SB;
  AM16(Cc[15], w327, h9b);
  SB;
  AM16N(Cc[4], w328, h9a, w329);
  SB;
  s16x8 w331 = TR2(ldsV3_2, 24704, 30848);
  SB;
  AM16(Cc[16], w328, h9b);
  LOAD4(h14b, vhB, srdWS, sH + 262144, 0, " sc1");
  SB;
  AM16(Cc[5], w329, h9a);
  SB;
  s16x8 w332 = TR2(ldsV0_2, 24832, 30976);
  SB;
  AM16(Cc[17], w329, h9b);
  u32 voffO0; { const u32 bb = w * 3072 + 0 + vl16; const u32 rw = bb / 192; voffO0 = OPQ(rw * 1152 + (bb - rw * 192)); }
  SB;
  AM16N(Cc[6], w330, h9a, w331);
  SB;
  s16x8 w333 = TR2(ldsV1_2, 24832, 30976);
  SB;
  AM16(Cc[18], w330, h9b);
  DMA2(lW, 98304, voffV0, srdW2, sV, 1179648);
  SB;
  AM16(Cc[7], w331, h9a);
  SB;
  s16x8 w334 = TR2(ldsV2_2, 24832, 30976);
  SB;
  AM16(Cc[19], w331, h9b);
  SB;
  AM16N(Cc[8], w332, h9a, w333);
  SB;
  s16x8 w335 = TR2(ldsV3_2, 24832, 30976);
  SB;
  AM16(Cc[20], w332, h9b);
  DMA2(lW, 99328, voffV1, srdW2, sV, 1179648);
  SB;
  AM16(Cc[9], w333, h9a);
  SB;
  s16x8 w336 = TR2(ldsV0_0, 0, 6144);
  SB;
  AM16(Cc[21], w333, h9b);
  SB;
  AM16N(Cc[10], w334, h9a, w335);
  SB;
  s16x8 w337 = TR2(ldsV1_0, 0, 6144);
  SB;
  AM16(Cc[22], w334, h9b);
  DMA2(lW, 100352, voffV2, srdW2, sV, 1179648);
  SB;
  AM16(Cc[11], w335, h9a);
  SB;
  s16x8 w338 = TR2(ldsV2_0, 0, 6144);
  SB;
  AM16(Cc[23], w335, h9b);
  u32 voffO1; { const u32 bb = w * 3072 + 1024 + vl16; const u32 rw = bb / 192; voffO1 = OPQ(rw * 1152 + (bb - rw * 192)); }
  SB;
  SB; asm volatile("s_waitcnt vmcnt(10)\n\ts_barrier" : "+v"(pl6), "+v"(h10a), "+v"(h10b) :: "memory"); SB;
  POLL2(pl6, voffHP, 6, 6);
  AM16N(Cc[0], w336, h10a, w337);
  SB;
  s16x8 w339 = TR2(ldsV3_0, 0, 6144);
  SB;
  AM16(Cc[12], w336, h10b);
  SB;
  AM16(Cc[1], w337, h10a);
  SB;
  s16x8 w340 = TR2(ldsV0_0, 128, 6272);
  SB;
  AM16(Cc[13], w337, h10b);
  SB;
  AM16N(Cc[2], w338, h10a, w339);
  SB;
  s16x8 w341 = TR2(ldsV1_0, 128, 6272);
  SB;
  AM16(Cc[14], w338, h10b);
  LOAD4(h15a, vhA, srdWS, sH + 81920, 0, " sc1");
  SB;
  AM16(Cc[3], w339, h10a);
  SB;
  s16x8 w342 = TR2(ldsV2_0, 128, 6272);
  SB;
  AM16(Cc[15], w339, h10b);
  SB;
  AM16N(Cc[4], w340, h10a, w341);
  SB;
  s16x8 w343 = TR2(ldsV3_0, 128, 6272);
  SB;
  AM16(Cc[16], w340, h10b);
  LOAD4(h15b, vhB, srdWS, sH + 81920, 0, " sc1");
  SB;
  AM16(Cc[5], w341, h10a);
  SB;
  s16x8 w344 = TR2(ldsV0_0, 256, 6400);
  SB;
  AM16(Cc[17], w341, h10b);
  u32 voffO2; { const u32 bb = w * 3072 + 2048 + vl16; const u32 rw = bb / 192; voffO2 = OPQ(rw * 1152 + (bb - rw * 192)); }
  SB;
  AM16N(Cc[6], w342, h10a, w343);
  SB;
  s16x8 w345 = TR2(ldsV1_0, 256, 6400);
  SB;
  AM16(Cc[18], w342, h10b);
  DMA2(lW, 122880, voffV0, srdW2, sV, 368640);
  SB;
  AM16(Cc[7], w343, h10a);
  SB;
  s16x8 w346 = TR2(ldsV2_0, 256, 6400);
  SB;
  AM16(Cc[19], w343, h10b);
  SB;
  AM16N(Cc[8], w344, h10a, w345);
  SB;
  s16x8 w347 = TR2(ldsV3_0, 256, 6400);
  SB;
  AM16(Cc[20], w344, h10b);
  DMA2(lW, 123904, voffV1, srdW2, sV, 368640);
  SB;
  AM16(Cc[9], w345, h10a);
  SB;
  s16x8 w348 = TR2(ldsV0_0, 24576, 30720);
  SB;
  AM16(Cc[21], w345, h10b);
  const u32 sO = 384 * c + 192 * (j & 1), voffOP = ln >= 2 ? OOB : 1048576 + (((r * 4 + rg) * 8 + 2 * th + cg + 4 * ln) * 128), sOi = 16777216 + (r * 4 + rg) * 40960 + (4 * th + 2 * cg) * 1024; u32x4 of0a, of0b, of1a, of1b, of2a, of2b, of3a, of3b, of4a, of4b;
  SB;
  AM16N(Cc[10], w346, h10a, w347);
  SB;
  s16x8 w349 = TR2(ldsV1_0, 24576, 30720);
  SB;
  AM16(Cc[22], w346, h10b);
  DMA2(lW, 124928, voffV2, srdW2, sV, 368640);
  SB;
  AM16(Cc[11], w347, h10a);
  SB;
  s16x8 w350 = TR2(ldsV2_0, 24576, 30720);
  SB;
  AM16(Cc[23], w347, h10b);
  SB;
  SB; asm volatile("s_barrier" : "+v"(h11a), "+v"(h11b) :: "memory"); SB;
  AM16N(Cc[0], w348, h11a, w349);
  SB;
  s16x8 w351 = TR2(ldsV3_0, 24576, 30720);
  SB;
  AM16(Cc[12], w348, h11b);
  SB;
  AM16(Cc[1], w349, h11a);
  SB;
  s16x8 w352 = TR2(ldsV0_0, 24704, 30848);
  SB;
  AM16(Cc[13], w349, h11b);
  SB;
  AM16N(Cc[2], w350, h11a, w351);
  SB;
  s16x8 w353 = TR2(ldsV1_0, 24704, 30848);
  SB;
  AM16(Cc[14], w350, h11b);
  LOAD4(h16a, vhA, srdWS, sH + 180224, 0, " sc1");
  SB;
  AM16(Cc[3], w351, h11a);
  SB;
  s16x8 w354 = TR2(ldsV2_0, 24704, 30848);
  SB;
  AM16(Cc[15], w351, h11b);
  SB;
  AM16N(Cc[4], w352, h11a, w353);
  SB;
  s16x8 w355 = TR2(ldsV3_0, 24704, 30848);
  SB;
  AM16(Cc[16], w352, h11b);
  LOAD4(h16b, vhB, srdWS, sH + 180224, 0, " sc1");
  SB;
  AM16(Cc[5], w353, h11a);
  SB;
  s16x8 w356 = TR2(ldsV0_0, 24832, 30976);
  SB;
  AM16(Cc[17], w353, h11b);
  SB;
  AM16N(Cc[6], w354, h11a, w355);
  SB;
  s16x8 w357 = TR2(ldsV1_0, 24832, 30976);
  SB;
  AM16(Cc[18], w354, h11b);
  DMA2(lW, 0, voffV0, srdW2, sV, 811008);
  SB;
  AM16(Cc[7], w355, h11a);
  SB;
  s16x8 w358 = TR2(ldsV2_0, 24832, 30976);
  SB;
  AM16(Cc[19], w355, h11b);
  SB;
  AM16N(Cc[8], w356, h11a, w357);
  SB;
  s16x8 w359 = TR2(ldsV3_0, 24832, 30976);
  SB;
  AM16(Cc[20], w356, h11b);
  DMA2(lW, 1024, voffV1, srdW2, sV, 811008);
  SB;
  AM16(Cc[9], w357, h11a);
  SB;
  s16x8 w360 = TR2(ldsV0_1, 0, 6144);
  SB;
  AM16(Cc[21], w357, h11b);
  SB;
  AM16N(Cc[10], w358, h11a, w359);
  SB;
  s16x8 w361 = TR2(ldsV1_1, 0, 6144);
  SB;
  AM16(Cc[22], w358, h11b);
  DMA2(lW, 2048, voffV2, srdW2, sV, 811008);
  SB;
  AM16(Cc[11], w359, h11a);
  SB;
  s16x8 w362 = TR2(ldsV2_1, 0, 6144);
  SB;
  AM16(Cc[23], w359, h11b);
  SB;
  SB; asm volatile("s_waitcnt vmcnt(15)\n\ts_barrier" : "+v"(h12a), "+v"(h12b) :: "memory"); SB;
  AM16N(Cc[0], w360, h12a, w361);
  SB;
  s16x8 w363 = TR2(ldsV3_1, 0, 6144);
  SB;
  AM16(Cc[12], w360, h12b);
  SB;
  AM16(Cc[1], w361, h12a);
  SB;
  s16x8 w364 = TR2(ldsV0_1, 128, 6272);
  SB;
  AM16(Cc[13], w361, h12b);
  LOAD4(h17a, vhA, srdWS, sH + 278528, 0, " sc1");
  SB;
  AM16N(Cc[2], w362, h12a, w363);
  SB;
  s16x8 w365 = TR2(ldsV1_1, 128, 6272);
  SB;
  AM16(Cc[14], w362, h12b);
  SB;
  AM16(Cc[3], w363, h12a);
  SB;
  s16x8 w366 = TR2(ldsV2_1, 128, 6272);
  SB;
  AM16(Cc[15], w363, h12b);
  LOAD4(h17b, vhB, srdWS, sH + 278528, 0, " sc1");
  SB;
  AM16N(Cc[4], w364, h12a, w365);
  SB;
  s16x8 w367 = TR2(ldsV3_1, 128, 6272);
  SB;
  AM16(Cc[16], w364, h12b);
  DMA2(lW, 24576, voffV0, srdW2, sV, 1253376);
  SB;
  AM16(Cc[5], w365, h12a);
  SB;
  s16x8 w368 = TR2(ldsV0_1, 256, 6400);
  SB;
  AM16(Cc[17], w365, h12b);
  DMA2(lW, 25600, voffV1, srdW2, sV, 1253376);
  SB;
  AM16N(Cc[6], w366, h12a, w367);
  SB;
  s16x8 w369 = TR2(ldsV1_1, 256, 6400);
  SB;
  AM16(Cc[18], w366, h12b);
  SB;
  AM16(Cc[7], w367, h12a);
  SB;
  s16x8 w370 = TR2(ldsV2_1, 256, 6400);
  SB;
  AM16(Cc[19], w367, h12b);
  DMA2(lW, 26624, voffV2, srdW2, sV, 1253376);
  SB;
  AM16N(Cc[8], w368, h12a, w369);
  SB;
  s16x8 w371 = TR2(ldsV3_1, 256, 6400);
  SB;
  AM16(Cc[20], w368, h12b);
  DMA_C(cg, eq, 0, lW + 147456, voffO0, srdO, sO + 589824);
  SB;
  AM16(Cc[9], w369, h12a);
  SB;
  s16x8 w372 = TR2(ldsV0_1, 24576, 30720);
  SB;
  AM16(Cc[21], w369, h12b);
  DMA_C(cg, eq, 0, lW + 148480, voffO1, srdO, sO + 589824);
  SB;
  AM16N(Cc[10], w370, h12a, w371);
  SB;
  s16x8 w373 = TR2(ldsV1_1, 24576, 30720);
  SB;
  AM16(Cc[22], w370, h12b);
  SB;
  AM16(Cc[11], w371, h12a);
  SB;
  s16x8 w374 = TR2(ldsV2_1, 24576, 30720);
  SB;
  AM16(Cc[23], w371, h12b);
  DMA_C(cg, eq, 0, lW + 149504, voffO2, srdO, sO + 589824);
  SB;
  SB; asm volatile("s_waitcnt vmcnt(15)\n\ts_barrier" : "+v"(h13a), "+v"(h13b) :: "memory"); SB;
  u32x2 plo;
  LOAD2(plo, voffOP, srdWS, 0, 0, " sc1");
  AM16N(Cc[0], w372, h13a, w373);
  SB;
  s16x8 w375 = TR2(ldsV3_1, 24576, 30720);
  SB;
  AM16(Cc[12], w372, h13b);
  SB;
  AM16(Cc[1], w373, h13a);
  SB;
  s16x8 w376 = TR2(ldsV0_1, 24704, 30848);
  SB;
  AM16(Cc[13], w373, h13b);
  SB;
  AM16N(Cc[2], w374, h13a, w375);
  SB;
  s16x8 w377 = TR2(ldsV1_1, 24704, 30848);
  SB;
  AM16(Cc[14], w374, h13b);
  DMA2(lW, 49152, voffO0, srdO, sO, 0);
  SB;
  AM16(Cc[3], w375, h13a);
  SB;
  s16x8 w378 = TR2(ldsV2_1, 24704, 30848);
  SB;
  AM16(Cc[15], w375, h13b);
  SB;
  AM16N(Cc[4], w376, h13a, w377);
  SB;
  s16x8 w379 = TR2(ldsV3_1, 24704, 30848);
  SB;
  AM16(Cc[16], w376, h13b);
  SB;
  AM16(Cc[5], w377, h13a);
  SB;
  s16x8 w380 = TR2(ldsV0_1, 24832, 30976);
  SB;
  AM16(Cc[17], w377, h13b);
  SB;
  AM16N(Cc[6], w378, h13a, w379);
  SB;
  s16x8 w381 = TR2(ldsV1_1, 24832, 30976);
  SB;
  AM16(Cc[18], w378, h13b);
  DMA2(lW, 50176, voffO1, srdO, sO, 0);
  SB;
  AM16(Cc[7], w379, h13a);
  SB;
  s16x8 w382 = TR2(ldsV2_1, 24832, 30976);
  SB;
  AM16(Cc[19], w379, h13b);
  SB;
  AM16N(Cc[8], w380, h13a, w381);
  SB;
  s16x8 w383 = TR2(ldsV3_1, 24832, 30976);
  SB;
  AM16(Cc[20], w380, h13b);
  SB;
  AM16(Cc[9], w381, h13a);
  SB;
  s16x8 w384 = TR2(ldsV0_2, 0, 6144);
  SB;
  AM16(Cc[21], w381, h13b);
  SB;
  AM16N(Cc[10], w382, h13a, w383);
  SB;
  s16x8 w385 = TR2(ldsV1_2, 0, 6144);
  SB;
  AM16(Cc[22], w382, h13b);
  DMA2(lW, 51200, voffO2, srdO, sO, 0);
  SB;
  AM16(Cc[11], w383, h13a);
  SB;
  s16x8 w386 = TR2(ldsV2_2, 0, 6144);
  SB;
  AM16(Cc[23], w383, h13b);
  SB;
  SB; asm volatile("s_waitcnt vmcnt(14)\n\ts_barrier" : "+v"(h14a), "+v"(h14b) :: "memory"); SB;
  AM16N(Cc[0], w384, h14a, w385);
  SB;
  s16x8 w387 = TR2(ldsV3_2, 0, 6144);
  SB;
  AM16(Cc[12], w384, h14b);
  SB;
  AM16(Cc[1], w385, h14a);
  SB;
  s16x8 w388 = TR2(ldsV0_2, 128, 6272);
  SB;
  AM16(Cc[13], w385, h14b);
  SB;
  AM16N(Cc[2], w386, h14a, w387);
  SB;
  s16x8 w389 = TR2(ldsV1_2, 128, 6272);
  SB;
  AM16(Cc[14], w386, h14b);
  DMA2(lW, 73728, voffO0, srdO, sO, 147456);
  SB;
  AM16(Cc[3], w387, h14a);
  SB;
  s16x8 w390 = TR2(ldsV2_2, 128, 6272);
  SB;
  AM16(Cc[15], w387, h14b);
  SB;
  AM16N(Cc[4], w388, h14a, w389);
  SB;
  s16x8 w391 = TR2(ldsV3_2, 128, 6272);
  SB;
  AM16(Cc[16], w388, h14b);
  SB;
  AM16(Cc[5], w389, h14a);
  SB;
  s16x8 w392 = TR2(ldsV0_2, 256, 6400);
  SB;
  AM16(Cc[17], w389, h14b);
  SB;
  AM16N(Cc[6], w390, h14a, w391);
  SB;
  s16x8 w393 = TR2(ldsV1_2, 256, 6400);
  SB;
  AM16(Cc[18], w390, h14b);
  DMA2(lW, 74752, voffO1, srdO, sO, 147456);
  SB;
  AM16(Cc[7], w391, h14a);
  SB;
  s16x8 w394 = TR2(ldsV2_2, 256, 6400);
  SB;
  AM16(Cc[19], w391, h14b);
  SB;
  AM16N(Cc[8], w392, h14a, w393);
  SB;
  s16x8 w395 = TR2(ldsV3_2, 256, 6400);
  SB;
  AM16(Cc[20], w392, h14b);
  SB;
  AM16(Cc[9], w393, h14a);
  SB;
  s16x8 w396 = TR2(ldsV0_2, 24576, 30720);
  SB;
  AM16(Cc[21], w393, h14b);
  SB;
  AM16N(Cc[10], w394, h14a, w395);
  SB;
  s16x8 w397 = TR2(ldsV1_2, 24576, 30720);
  SB;
  AM16(Cc[22], w394, h14b);
  DMA2(lW, 75776, voffO2, srdO, sO, 147456);
  SB;
  AM16(Cc[11], w395, h14a);
  SB;
  s16x8 w398 = TR2(ldsV2_2, 24576, 30720);
  SB;
  AM16(Cc[23], w395, h14b);
  SB;
  SB; asm volatile("s_waitcnt vmcnt(6)\n\ts_barrier" : "+v"(plo), "+v"(h15a), "+v"(h15b) :: "memory"); SB;
  POLL2(plo, voffOP, 1, 7);
  AM16N(Cc[0], w396, h15a, w397);
  SB;
  s16x8 w399 = TR2(ldsV3_2, 24576, 30720);
  SB;
  AM16(Cc[12], w396, h15b);
  SB;
  AM16(Cc[1], w397, h15a);
  SB;
  s16x8 w400 = TR2(ldsV0_2, 24704, 30848);
  SB;
  AM16(Cc[13], w397, h15b);
  DMA2(lW, 98304, voffO0, srdO, sO, 294912);
  SB;
  AM16N(Cc[2], w398, h15a, w399);
  SB;
  s16x8 w401 = TR2(ldsV1_2, 24704, 30848);
  SB;
  AM16(Cc[14], w398, h15b);
  SB;
  AM16(Cc[3], w399, h15a);
  SB;
  s16x8 w402 = TR2(ldsV2_2, 24704, 30848);
  SB;
  AM16(Cc[15], w399, h15b);
  DMA2(lW, 99328, voffO1, srdO, sO, 294912);
  SB;
  AM16N(Cc[4], w400, h15a, w401);
  SB;
  s16x8 w403 = TR2(ldsV3_2, 24704, 30848);
  SB;
  AM16(Cc[16], w400, h15b);
  SB;
  AM16(Cc[5], w401, h15a);
  SB;
  s16x8 w404 = TR2(ldsV0_2, 24832, 30976);
  SB;
  AM16(Cc[17], w401, h15b);
  DMA2(lW, 100352, voffO2, srdO, sO, 294912);
  SB;
  AM16N(Cc[6], w402, h15a, w403);
  SB;
  s16x8 w405 = TR2(ldsV1_2, 24832, 30976);
  SB;
  AM16(Cc[18], w402, h15b);
  SB;
  AM16(Cc[7], w403, h15a);
  SB;
  s16x8 w406 = TR2(ldsV2_2, 24832, 30976);
  SB;
  AM16(Cc[19], w403, h15b);
  LOAD4(of0a, vl16, srdWS, sOi + 0, 0, " sc1");
  SB;
  AM16N(Cc[8], w404, h15a, w405);
  SB;
  s16x8 w407 = TR2(ldsV3_2, 24832, 30976);
  SB;
  AM16(Cc[20], w404, h15b);
  SB;
  AM16(Cc[9], w405, h15a);
  SB;
  s16x8 w408 = TR2(ldsV0_0, 0, 6144);
  SB;
  AM16(Cc[21], w405, h15b);
  LOAD4(of0b, vl16, srdWS, sOi + 0, 1024, " sc1");
  SB;
  AM16N(Cc[10], w406, h15a, w407);
  SB;
  s16x8 w409 = TR2(ldsV1_0, 0, 6144);
  SB;
  AM16(Cc[22], w406, h15b);
  SB;
  AM16(Cc[11], w407, h15a);
  SB;
  s16x8 w410 = TR2(ldsV2_0, 0, 6144);
  SB;
  AM16(Cc[23], w407, h15b);
  LOAD4(of1a, vl16, srdWS, sOi + 8192, 0, " sc1");
  SB;
  SB; asm volatile("s_barrier" : "+v"(h16a), "+v"(h16b) :: "memory"); SB;
  AM16N(Cc[0], w408, h16a, w409);
  SB;
  s16x8 w411 = TR2(ldsV3_0, 0, 6144);
  SB;
  AM16(Cc[12], w408, h16b);
  SB;
  AM16(Cc[1], w409, h16a);
  SB;
  s16x8 w412 = TR2(ldsV0_0, 128, 6272);
  SB;
  AM16(Cc[13], w409, h16b);
  DMA2(lW, 122880, voffO0, srdO, sO, 442368);
  SB;
  AM16N(Cc[2], w410, h16a, w411);
  SB;
  s16x8 w413 = TR2(ldsV1_0, 128, 6272);
  SB;
  AM16(Cc[14], w410, h16b);
  SB;
  AM16(Cc[3], w411, h16a);
  SB;
  s16x8 w414 = TR2(ldsV2_0, 128, 6272);
  SB;
  AM16(Cc[15], w411, h16b);
  DMA2(lW, 123904, voffO1, srdO, sO, 442368);
  SB;
  AM16N(Cc[4], w412, h16a, w413);
  SB;
  s16x8 w415 = TR2(ldsV3_0, 128, 6272);
  SB;
  AM16(Cc[16], w412, h16b);
  SB;
  AM16(Cc[5], w413, h16a);
  SB;
  s16x8 w416 = TR2(ldsV0_0, 256, 6400);
  SB;
  AM16(Cc[17], w413, h16b);
  DMA2(lW, 124928, voffO2, srdO, sO, 442368);
  SB;
  AM16N(Cc[6], w414, h16a, w415);
  SB;
  s16x8 w417 = TR2(ldsV1_0, 256, 6400);
  SB;
  AM16(Cc[18], w414, h16b);
  SB;
  AM16(Cc[7], w415, h16a);
  SB;
  s16x8 w418 = TR2(ldsV2_0, 256, 6400);
  SB;
  AM16(Cc[19], w415, h16b);
  LOAD4(of1b, vl16, srdWS, sOi + 8192, 1024, " sc1");
  SB;
  AM16N(Cc[8], w416, h16a, w417);
  SB;
  s16x8 w419 = TR2(ldsV3_0, 256, 6400);
  SB;
  AM16(Cc[20], w416, h16b);
  SB;
  AM16(Cc[9], w417, h16a);
  SB;
  s16x8 w420 = TR2(ldsV0_0, 24576, 30720);
  SB;
  AM16(Cc[21], w417, h16b);
  LOAD4(of2a, vl16, srdWS, sOi + 16384, 0, " sc1");
  SB;
  AM16N(Cc[10], w418, h16a, w419);
  SB;
  s16x8 w421 = TR2(ldsV1_0, 24576, 30720);
  SB;
  AM16(Cc[22], w418, h16b);
  SB;
  AM16(Cc[11], w419, h16a);
  SB;
  s16x8 w422 = TR2(ldsV2_0, 24576, 30720);
  SB;
  AM16(Cc[23], w419, h16b);
  LOAD4(of2b, vl16, srdWS, sOi + 16384, 1024, " sc1");
  SB;
  SB; asm volatile("s_barrier" : "+v"(h17a), "+v"(h17b) :: "memory"); SB;
  u32x4 tw0, tw1, tw2, tw3, tw4, tw5; const Sel se = mk_sel(ln);
  AM16N(Cc[0], w420, h17a, w421);
  SB;
  s16x8 w423 = TR2(ldsV3_0, 24576, 30720);
  SB;
  AM16(Cc[12], w420, h17b);
  SB;
  AM16(Cc[1], w421, h17a);
  SB;
  s16x8 w424 = TR2(ldsV0_0, 24704, 30848);
  SB;
  AM16(Cc[13], w421, h17b);
  SB;
  AM16N(Cc[2], w422, h17a, w423);
  SB;
  s16x8 w425 = TR2(ldsV1_0, 24704, 30848);
  SB;
  AM16(Cc[14], w422, h17b);
  LOAD4(of3a, vl16, srdWS, sOi + 24576, 0, " sc1");
  SB;
  AM16(Cc[3], w423, h17a);
  SB;
  s16x8 w426 = TR2(ldsV2_0, 24704, 30848);
  SB;
  AM16(Cc[15], w423, h17b);
  SB;
  AM16N(Cc[4], w424, h17a, w425);
  SB;
  s16x8 w427 = TR2(ldsV3_0, 24704, 30848);
  SB;
  AM16(Cc[16], w424, h17b);
  tw0 = pack2(Cc[12], Cc[13]);
  SB;
  AM16(Cc[5], w425, h17a);
  SB;
  s16x8 w428 = TR2(ldsV0_0, 24832, 30976);
  SB;
  AM16(Cc[17], w425, h17b);
  LOAD4(of3b, vl16, srdWS, sOi + 24576, 1024, " sc1");
  SB;
  AM16N(Cc[6], w426, h17a, w427);
  SB;
  s16x8 w429 = TR2(ldsV1_0, 24832, 30976);
  SB;
  AM16(Cc[18], w426, h17b);
  tw1 = pack2(Cc[14], Cc[15]);
  SB;
  AM16(Cc[7], w427, h17a);
  SB;
  s16x8 w430 = TR2(ldsV2_0, 24832, 30976);
  SB;
  AM16(Cc[19], w427, h17b);
  LOAD4(of4a, vl16, srdWS, sOi + 32768, 0, " sc1");
  SB;
  AM16N(Cc[8], w428, h17a, w429);
  SB;
  s16x8 w431 = TR2(ldsV3_0, 24832, 30976);
  SB;
  AM16(Cc[20], w428, h17b);
  tw2 = pack2(Cc[16], Cc[17]);
  SB;
  AM16(Cc[9], w429, h17a);
  SB;
  SB;
  AM16(Cc[21], w429, h17b);
  SB;
  AM16N(Cc[10], w430, h17a, w431);
  SB;
  SB;
  AM16(Cc[22], w430, h17b);
  tw3 = pack2(Cc[18], Cc[19]);
  LOAD4(of4b, vl16, srdWS, sOi + 32768, 1024, " sc1");
  SB;
  AM16(Cc[11], w431, h17a);
  SB;
  SB;
  AM16(Cc[23], w431, h17b);
  SB;
  const u32 ldsTr = OPQ((cg ? 24576 : 0) + rg * 6144 + vl16), ldsTp = OPQ((cg ? 0 : 24576) + rg * 6144 + vl16);
  asm volatile("s_nop 7" : "+v"(Cc[11]), "+v"(Cc[23]));
  tw4 = pack2(Cc[20], Cc[21]);
  tw5 = pack2(Cc[22], Cc[23]);
  SB; asm volatile("s_barrier" : "+v"(tw0), "+v"(tw1), "+v"(tw2), "+v"(tw3), "+v"(tw4), "+v"(tw5) :: "memory"); SB;
  *(LAS u32x4*)(size_t)(ldsTr + 0) = tw0;
  *(LAS u32x4*)(size_t)(ldsTr + 1024) = tw1;
  *(LAS u32x4*)(size_t)(ldsTr + 2048) = tw2;
  *(LAS u32x4*)(size_t)(ldsTr + 3072) = tw3;
  *(LAS u32x4*)(size_t)(ldsTr + 4096) = tw4;
  *(LAS u32x4*)(size_t)(ldsTr + 5120) = tw5;
  const u32 sPX = 4194304 + ((r * 3 + c) * 16 + j) * 12288 + ((2 * rg + cg) & 3) * 3072 + (rg >> 1) * 98304;
  const u32 v0 = (fin | (j & 1)) ? vl16 : OOB, v1 = (fin | (~j & 1)) ? vl16 : OOB;
  SB; asm volatile("s_waitcnt vmcnt(7) lgkmcnt(0)\n\ts_barrier" :  :: "memory"); SB;
  u32x4 tv0 = *(LAS u32x4*)(size_t)(ldsTp + 0), tv1 = *(LAS u32x4*)(size_t)(ldsTp + 1024), tv2 = *(LAS u32x4*)(size_t)(ldsTp + 2048), tv3 = *(LAS u32x4*)(size_t)(ldsTp + 3072), tv4 = *(LAS u32x4*)(size_t)(ldsTp + 4096), tv5 = *(LAS u32x4*)(size_t)(ldsTp + 5120);
  madd(Cc[0], Cc[1], tv0, se);
  madd(Cc[2], Cc[3], tv1, se);
  madd(Cc[4], Cc[5], tv2, se);
  madd(Cc[6], Cc[7], tv3, se);
  madd(Cc[8], Cc[9], tv4, se);
  madd(Cc[10], Cc[11], tv5, se);
  SB;
  u32 ldsO, ldsOa, ldsOb; { const int r0 = 144 * rg - 16 + 16 * (g4 >> 1) + 4 * (g4 & 1) + q4; const u32 sw = (ln & 3) * 8; ldsO = OPQ(49152 + (r0 + 32) * 192 + sw); ldsOa = OPQ(49152 + (r0 < 0 ? 0 : r0) * 192 + sw); ldsOb = OPQ(49152 + (r0 + 8 < 0 ? 0 : r0 + 8) * 192 + sw); }
  const u32x4 pv0 = pack2(Cc[0], Cc[1]);
  STORE4(pv0, v0, srdWS, sPX + 0, 0);
  const u32x4 pv1 = pack2(Cc[2], Cc[3]);
  STORE4(pv1, v0, srdWS, sPX + 0, 1024);
  const u32x4 pv2 = pack2(Cc[4], Cc[5]);
  STORE4(pv2, v0, srdWS, sPX + 0, 2048);
  const u32x4 pv3 = pack2(Cc[6], Cc[7]);
  STORE4(pv3, v1, srdWS, sPX + 49152, 0);
  const u32x4 pv4 = pack2(Cc[8], Cc[9]);
  STORE4(pv4, v1, srdWS, sPX + 49152, 1024);
  const u32x4 pv5 = pack2(Cc[10], Cc[11]);
  STORE4(pv5, v1, srdWS, sPX + 49152, 2048);
  SB; asm volatile("s_waitcnt vmcnt(6)" : "+v"(of0a), "+v"(of0b), "+v"(of1a), "+v"(of1b), "+v"(of2a), "+v"(of2b), "+v"(of3a), "+v"(of3b), "+v"(of4a), "+v"(of4b) :: "memory"); SB;
  const s16x8 uf0 = TR2B(ldsOa, 0, ldsOb, 0);
  const s16x8 uf1 = TR2B(ldsOa, 32, ldsOb, 32);
  const s16x8 uf2 = TR2B(ldsOa, 64, ldsOb, 64);
  f32x4 Sa[12]; u32x4 xv[2], ra[10]; u32x2 plp; FinalIO io;
  AM16Z(Sa[0], uf0, of0a); SB;
  const s16x8 uf3 = TR2B(ldsOa, 96, ldsOb, 96);
  SB; AM16Z(Sa[6], uf0, of0b);
  SB;
  AM16Z(Sa[1], uf1, of0a); SB;
  const s16x8 uf4 = TR2B(ldsOa, 128, ldsOb, 128);
  SB; AM16Z(Sa[7], uf1, of0b);
  SB;
  AM16Z(Sa[2], uf2, of0a); SB;
  const s16x8 uf5 = TR2B(ldsOa, 160, ldsOb, 160);
  SB; AM16Z(Sa[8], uf2, of0b);
  SB;
  AM16Z(Sa[3], uf3, of0a); SB;
  const s16x8 uf6 = TR2(ldsO, 0, 1536);
  SB; AM16Z(Sa[9], uf3, of0b);
  SB;
  AM16Z(Sa[4], uf4, of0a); SB;
  const s16x8 uf7 = TR2(ldsO, 32, 1568);
  SB; AM16Z(Sa[10], uf4, of0b);
  SB;
  AM16Z(Sa[5], uf5, of0a); SB;
  const s16x8 uf8 = TR2(ldsO, 64, 1600);
  SB; AM16Z(Sa[11], uf5, of0b);
  SB;
  SB; asm volatile("s_barrier" :  :: "memory"); SB;
  AM16(Sa[0], uf6, of1a); SB;
  const s16x8 uf9 = TR2(ldsO, 96, 1632);
  SB; AM16(Sa[6], uf6, of1b);
  SB;
  AM16(Sa[1], uf7, of1a); SB;
  const s16x8 uf10 = TR2(ldsO, 128, 1664);
  SB; AM16(Sa[7], uf7, of1b);
  SB;
  final_prep(io, r, j, c, rg, cg, ln);
  AM16(Sa[2], uf8, of1a); SB;
  const s16x8 uf11 = TR2(ldsO, 160, 1696);
  SB; AM16(Sa[8], uf8, of1b);
  SB;
  AM16(Sa[3], uf9, of1a); SB;
  const s16x8 uf12 = TR2(ldsO, 6144, 7680);
  SB; AM16(Sa[9], uf9, of1b);
  SB;
  AM16(Sa[4], uf10, of1a); SB;
  const s16x8 uf13 = TR2(ldsO, 6176, 7712);
  SB; AM16(Sa[10], uf10, of1b);
  SB;
  AM16(Sa[5], uf11, of1a); SB;
  const s16x8 uf14 = TR2(ldsO, 6208, 7744);
  SB; AM16(Sa[11], uf11, of1b);
  SB;
  AM16(Sa[0], uf12, of2a); SB;
  const s16x8 uf15 = TR2(ldsO, 6240, 7776);
  SB; AM16(Sa[6], uf12, of2b);
  SB;
  AM16(Sa[1], uf13, of2a); SB;
  const s16x8 uf16 = TR2(ldsO, 6272, 7808);
  SB; AM16(Sa[7], uf13, of2b);
  SB;
  AM16(Sa[2], uf14, of2a); SB;
  const s16x8 uf17 = TR2(ldsO, 6304, 7840);
  SB; AM16(Sa[8], uf14, of2b);
  SB;
  AM16(Sa[3], uf15, of2a); SB;
  const s16x8 uf18 = TR2(ldsO, 12288, 13824);
  SB; AM16(Sa[9], uf15, of2b);
  SB;
  AM16(Sa[4], uf16, of2a); SB;
  const s16x8 uf19 = TR2(ldsO, 12320, 13856);
  SB; AM16(Sa[10], uf16, of2b);
  SB;
  AM16(Sa[5], uf17, of2a); SB;
  const s16x8 uf20 = TR2(ldsO, 12352, 13888);
  SB; AM16(Sa[11], uf17, of2b);
  SB;
  SB; asm volatile("s_waitcnt vmcnt(0)" :  :: "memory"); SB;
  FLAG2(sPF);
  AM16(Sa[0], uf18, of3a); SB;
  const s16x8 uf21 = TR2(ldsO, 12384, 13920);
  SB; AM16(Sa[6], uf18, of3b);
  SB;
  AM16(Sa[1], uf19, of3a); SB;
  const s16x8 uf22 = TR2(ldsO, 12416, 13952);
  SB; AM16(Sa[7], uf19, of3b);
  LOAD4(xv[0], io.vo[0], srdX, io.so, 0, "");
  SB;
  AM16(Sa[2], uf20, of3a); SB;
  const s16x8 uf23 = TR2(ldsO, 12448, 13984);
  SB; AM16(Sa[8], uf20, of3b);
  SB;
  AM16(Sa[3], uf21, of3a); SB;
  const s16x8 uf24 = TR2(ldsO, 18432, 19968);
  SB; AM16(Sa[9], uf21, of3b);
  SB;
  AM16(Sa[4], uf22, of3a); SB;
  const s16x8 uf25 = TR2(ldsO, 18464, 20000);
  SB; AM16(Sa[10], uf22, of3b);
  LOAD4(xv[1], io.vo[1], srdX, io.so, 0, "");
  SB;
  AM16(Sa[5], uf23, of3a); SB;
  const s16x8 uf26 = TR2(ldsO, 18496, 20032);
  SB; AM16(Sa[11], uf23, of3b);
  SB;
  AM16(Sa[0], uf24, of4a); SB;
  const s16x8 uf27 = TR2(ldsO, 18528, 20064);
  SB; AM16(Sa[6], uf24, of4b);
  SB;
  AM16(Sa[1], uf25, of4a); SB;
  const s16x8 uf28 = TR2(ldsO, 18560, 20096);
  SB; AM16(Sa[7], uf25, of4b);
  SB;
  AM16(Sa[2], uf26, of4a); SB;
  const s16x8 uf29 = TR2(ldsO, 18592, 20128);
  SB; AM16(Sa[8], uf26, of4b);
  SB;
  AM16(Sa[3], uf27, of4a); SB;
  SB; AM16(Sa[9], uf27, of4b);
  SB;
  AM16(Sa[4], uf28, of4a); SB;
  SB; AM16(Sa[10], uf28, of4b);
  SB;
  AM16(Sa[5], uf29, of4a); SB;
  SB; AM16(Sa[11], uf29, of4b);
  SB;
  const u32 lsw = OPQ(rg * 1024 + vl16 + cg * 24576); asm volatile("s_nop 5" : "+v"(Sa[5]), "+v"(Sa[11]));
  *(LAS u32x4*)(size_t)(lsw + 0) = pack2(Sa[0], Sa[1]);
  *(LAS u32x4*)(size_t)(lsw + 4096) = pack2(Sa[2], Sa[3]);
  *(LAS u32x4*)(size_t)(lsw + 12288) = pack2(Sa[6], Sa[7]);
  SB;
  LOAD2(plp, io.vp, srdWS, 0, 0, " sc1");
  *(LAS u32x4*)(size_t)(lsw + 16384) = pack2(Sa[8], Sa[9]);
  *(LAS u32x4*)(size_t)(lsw + 8192) = pack2(Sa[4], Sa[5]);
  *(LAS u32x4*)(size_t)(lsw + 20480) = pack2(Sa[10], Sa[11]);
  SB; asm volatile("s_waitcnt lgkmcnt(0)\n\ts_barrier" ::: "memory"); SB;
  SB; asm volatile("s_waitcnt vmcnt(0)" : "+v"(plp), "+v"(xv[0]), "+v"(xv[1]) :: "memory"); SB;
  POLL2(plp, io.vp, 1, 9);
  LOAD4(ra[0], io.v0, srdWS, io.sP + ((j + 1) & 3) * 12288, 0, " sc1");
  LOAD4(ra[1], io.v1, srdWS, io.sP + ((j + 1) & 3) * 12288, 1024, " sc1");
  LOAD4(ra[2], io.v0, srdWS, io.sP + ((j + 2) & 3) * 12288, 0, " sc1");
  LOAD4(ra[3], io.v1, srdWS, io.sP + ((j + 2) & 3) * 12288, 1024, " sc1");
  LOAD4(ra[4], io.v0, srdWS, io.sP + ((j + 3) & 3) * 12288, 0, " sc1");
  LOAD4(ra[5], io.v1, srdWS, io.sP + ((j + 3) & 3) * 12288, 1024, " sc1");
  { const u32 lo_ = fin ? 122880 + vl16 : 98304 + ((2 * rg + cg) & 3) * 3072 + vl16;
    *(LAS u32x4*)(size_t)(lo_ + 0) = (j & 1) ? pv3 : pv0;
    *(LAS u32x4*)(size_t)(lo_ + 1024) = (j & 1) ? pv4 : pv1;
    *(LAS u32x4*)(size_t)(lo_ + 2048) = (j & 1) ? pv5 : pv2;
  }
  SB; asm volatile("s_waitcnt lgkmcnt(0)\n\ts_barrier" ::: "memory"); SB;
  ra[6] = *(LAS u32x4*)(size_t)(98304 + rg * 3072 + cg * 2048 + vl16); ra[7] = *(LAS u32x4*)(size_t)(98304 + rg * 3072 + 1024 + vl16);
  { const u32 lb = 161280 + cg * 128 + g4 * 8; const u32x2 b0 = *(LAS u32x2*)(size_t)lb, b1 = *(LAS u32x2*)(size_t)(lb + 32), b2_ = *(LAS u32x2*)(size_t)(lb + 64 - cg * 64), b3 = *(LAS u32x2*)(size_t)(lb + 96 - cg * 64); ra[8] = u32x4{b0[0], b0[1], b1[0], b1[1]}; ra[9] = u32x4{b2_[0], b2_[1], b3[0], b3[1]}; }
  final_stage(ra, xv, se, io, rg, cg, ln, srdOut, 0, 49152);
  } else {
    const u32 qW0 = (ln >> 3) * 3456 + ((ln & 7) ^ (((ln >> 4) & 1) << 2)) * 16 + 288 * j, sW = 24 * w * 3456;
    const f32x16 zero16 = {};
    const u32 qa = (ln & 31) * 64 + (((ln >> 5) ^ ((ln >> 1) & 7)) << 3);
  u32x4 xr[4][4]; u32x2 xt[4]; s16x8 xf[36]; u32x2 ep; u32 bia;
  u32 ft = 0, fx = 0;
  LOAD1(fx, pfo(ln, 9), srdX, sX + ((j * 4 + c) * 8 + w) * 1152);
  SB;
  const u32 lnsw = (2 * w + (ln >> 5)) & 7;
  const u32 lnw = (4 * w + (ln >> 4)) * 1152 + (l15 >> 3) * 128 + (((l15 & 7) ^ lnsw) << 4), lnt = (4 * w + (ln >> 4)) * 1152 + 1024 + (((l15 >> 1) ^ lnsw) << 4) + (ln & 1) * 8;
  const u32 ldsLnW0 = OPQ(lnw + 122880), ldsLnT0 = OPQ(lnt + 122880), ldsLnW1 = lnw, ldsLnT1 = lnt;
  const u32 hh = ln >> 5, t31 = ln & 31, q4 = l15 >> 2, xs = (t31 >> 1) & 7, lcA = (ln & 7) ^ (((ln >> 4) & 1) << 2);
  const u32 tb = (8 * hh + q4) * 128 + (((4 * cg + 2 * ((ln >> 4) & 1) + ((ln & 3) >> 1)) ^ ((q4 >> 1) << 2)) << 4) + (ln & 1) * 8;
  const u32 ldsT0 = OPQ(tb), ldsT1 = OPQ(tb + 49152), ldsT2 = OPQ(tb + 98304);
  const u32 ldsXf0 = t31 * 1152 + (((0 + hh) ^ xs) << 4), ldsXf1 = t31 * 1152 + (((2 + hh) ^ xs) << 4), ldsXf2 = t31 * 1152 + (((4 + hh) ^ xs) << 4), ldsXf3 = t31 * 1152 + (((6 + hh) ^ xs) << 4);
  const u32 voffH = (2 * rg + (t31 >> 4)) * 1024 + hh * 256 + l15 * 16;
  const u32 lW = w * 3072;
  const u32 sPF = WS_PFLAG + ((r * 4 + j) * 4 + c) * 1024 + w * 128;
  u32 pfv = 0;
  if (w == 7) {
    u64 pc; u32 len;
    asm volatile("s_getpc_b64 %0\n\tL_code_begin_%=:\n\ts_mov_b32 %1, L_code_end-L_code_begin_%=" : "=s"(pc), "=s"(len));
    const u32x4 srdC = mk_srd((const void*)(pc & ~127ull), len & ~127u);
    const u32 vo = ((idx & 15) * 64 + ln) * 128;
    asm volatile("buffer_load_dword %0, %1, %2, 0 offen" : "=v"(pfv) : "v"(vo), "s"(srdC) : "memory");
  }
  SB;
  SB; asm volatile("s_bitcmp1_b32 255, %[cr]\n\ts_cbranch_scc0 1f\n\ts_waitcnt vmcnt(0)\n1:\n\ts_barrier" : "+v"(fx) : [cr] "s"(w) : "memory", "scc"); SB;
  LOAD4(xr[0][0], voffX, srdX, sX + 0 * 36864, 0, "");
  LOAD4(xr[0][1], voffX, srdX, sX + 0 * 36864, 256, "");
  LOAD4(xr[0][2], voffX, srdX, sX + 0 * 36864, 512, "");
  LOAD4(xr[0][3], voffX, srdX, sX + 0 * 36864, 768, "");
  LOAD2(xt[0], voffXt, srdX, sX + 0 * 36864, 1024, "");
  asm volatile("s_barrier" ::: "memory");
  LOAD4(xr[1][0], voffX, srdX, sX + 1 * 36864, 0, "");
  LOAD4(xr[1][1], voffX, srdX, sX + 1 * 36864, 256, "");
  LOAD4(xr[1][2], voffX, srdX, sX + 1 * 36864, 512, "");
  LOAD4(xr[1][3], voffX, srdX, sX + 1 * 36864, 768, "");
  LOAD2(xt[1], voffXt, srdX, sX + 1 * 36864, 1024, "");
  LOAD4(xr[2][0], voffX, srdX, sX + 2 * 36864, 0, "");
  LOAD4(xr[2][1], voffX, srdX, sX + 2 * 36864, 256, "");
  LOAD4(xr[2][2], voffX, srdX, sX + 2 * 36864, 512, "");
  LOAD4(xr[2][3], voffX, srdX, sX + 2 * 36864, 768, "");
  LOAD2(xt[2], voffXt, srdX, sX + 2 * 36864, 1024, "");
  LOAD2_C(w, eq, 7, ep, pfo(ln, 1), srdWS, 0, 0, " sc1");
  SB; asm volatile("s_bitcmp1_b32 127, %[cr]\n\ts_cbranch_scc0 1f\n\ts_waitcnt vmcnt(10)\n1:\n\ts_bitcmp1_b32 128, %[cr]\n\ts_cbranch_scc0 2f\n\ts_waitcnt vmcnt(11)\n2:\n\t" : "+v"(xr[0][0]), "+v"(xr[0][1]), "+v"(xr[0][2]), "+v"(xr[0][3]), "+v"(xt[0]) : [cr] "s"(w) : "memory", "scc"); SB;
  LOAD4(xr[3][0], voffX, srdX, sX + 3 * 36864, 0, "");
  LOAD4(xr[3][1], voffX, srdX, sX + 3 * 36864, 256, "");
  LOAD4(xr[3][2], voffX, srdX, sX + 3 * 36864, 512, "");
  LOAD4(xr[3][3], voffX, srdX, sX + 3 * 36864, 768, "");
  LOAD2(xt[3], voffXt, srdX, sX + 3 * 36864, 1024, "");
  LOAD1K_C(wp, eq, 2, ft, ln >= 54 ? OOB : ((r & 15) * 36 + (w & 1) * 18 + ln % 18) * 3456 + (ln < 18 ? 0 : ln < 36 ? 256 : 1152), srdW, 1152 + 288 * j);
  float rl0, lc0; ln_stats(xr[0], xt[0], rl0, lc0); SB;
  ln_apply<0, 5>(xr[0], xt[0], rl0, lc0, ldsLnW0, ldsLnT0); SB;
  const u32 voffW0 = qW0 + 1152;
  DMA2(lW, 49152, voffW0, srdW, sW, 0);
  DMA2(lW, 50176, voffW0, srdW, sW, 27648);
  DMA2(lW, 51200, voffW0, srdW, sW, 55296);
  SB; asm volatile("s_bitcmp1_b32 79, %[cr]\n\ts_cbranch_scc0 1f\n\ts_waitcnt vmcnt(13)\n1:\n\ts_bitcmp1_b32 176, %[cr]\n\ts_cbranch_scc0 2f\n\ts_waitcnt vmcnt(14)\n2:\n\t" : "+v"(xr[1][0]), "+v"(xr[1][1]), "+v"(xr[1][2]), "+v"(xr[1][3]), "+v"(xt[1]) : [cr] "s"(w) : "memory", "scc"); SB;
  float rl1, lc1; ln_stats(xr[1], xt[1], rl1, lc1); SB;
  SB; asm volatile("s_waitcnt lgkmcnt(0)\n\ts_barrier" :  : [cr] "s"(w) : "memory", "scc"); SB;
  if (rg == 0) {
    const u32 xa0 = OPQ(ldsXf0 + 122880), xa1 = OPQ(ldsXf1 + 122880), xa2 = OPQ(ldsXf2 + 122880), xa3 = OPQ(ldsXf3 + 122880);
    xf[0] = *(LAS s16x8*)(size_t)(xa0 + 0);
    xf[1] = *(LAS s16x8*)(size_t)(xa1 + 0);
    xf[2] = *(LAS s16x8*)(size_t)(xa2 + 0);
    xf[3] = *(LAS s16x8*)(size_t)(xa3 + 0);
    xf[4] = *(LAS s16x8*)(size_t)(xa0 + 128);
    xf[5] = *(LAS s16x8*)(size_t)(xa1 + 128);
    xf[6] = *(LAS s16x8*)(size_t)(xa2 + 128);
    SB; ln_apply<0, 1>(xr[1], xt[1], rl1, lc1, ldsLnW1, ldsLnT1); SB;
    xf[7] = *(LAS s16x8*)(size_t)(xa3 + 128);
    xf[8] = *(LAS s16x8*)(size_t)(xa0 + 256);
    xf[9] = *(LAS s16x8*)(size_t)(xa1 + 256);
    xf[10] = *(LAS s16x8*)(size_t)(xa2 + 256);
    xf[11] = *(LAS s16x8*)(size_t)(xa3 + 256);
    xf[12] = *(LAS s16x8*)(size_t)(xa0 + 384);
    xf[13] = *(LAS s16x8*)(size_t)(xa1 + 384);
    SB; ln_apply<1, 2>(xr[1], xt[1], rl1, lc1, ldsLnW1, ldsLnT1); SB;
    xf[14] = *(LAS s16x8*)(size_t)(xa2 + 384);
    xf[15] = *(LAS s16x8*)(size_t)(xa3 + 384);
    xf[16] = *(LAS s16x8*)(size_t)(xa0 + 512);
    xf[17] = *(LAS s16x8*)(size_t)(xa1 + 512);
    xf[18] = *(LAS s16x8*)(size_t)(xa2 + 512);
    xf[19] = *(LAS s16x8*)(size_t)(xa3 + 512);
    xf[20] = *(LAS s16x8*)(size_t)(xa0 + 640);
    SB; ln_apply<2, 3>(xr[1], xt[1], rl1, lc1, ldsLnW1, ldsLnT1); SB;
    xf[21] = *(LAS s16x8*)(size_t)(xa1 + 640);
    xf[22] = *(LAS s16x8*)(size_t)(xa2 + 640);
    xf[23] = *(LAS s16x8*)(size_t)(xa3 + 640);
    xf[24] = *(LAS s16x8*)(size_t)(xa0 + 768);
    xf[25] = *(LAS s16x8*)(size_t)(xa1 + 768);
    xf[26] = *(LAS s16x8*)(size_t)(xa2 + 768);
    xf[27] = *(LAS s16x8*)(size_t)(xa3 + 768);
    SB; ln_apply<3, 4>(xr[1], xt[1], rl1, lc1, ldsLnW1, ldsLnT1); SB;
    xf[28] = *(LAS s16x8*)(size_t)(xa0 + 896);
    xf[29] = *(LAS s16x8*)(size_t)(xa1 + 896);
    xf[30] = *(LAS s16x8*)(size_t)(xa2 + 896);
    xf[31] = *(LAS s16x8*)(size_t)(xa3 + 896);
    xf[32] = *(LAS s16x8*)(size_t)(xa0 + 1024);
    xf[33] = *(LAS s16x8*)(size_t)(xa1 + 1024);
    xf[34] = *(LAS s16x8*)(size_t)(xa2 + 1024);
    xf[35] = *(LAS s16x8*)(size_t)(xa3 + 1024);
    SB; ln_apply<4, 5>(xr[1], xt[1], rl1, lc1, ldsLnW1, ldsLnT1); SB;
  } else { ln_apply<0, 5>(xr[1], xt[1], rl1, lc1, ldsLnW1, ldsLnT1); SB; }
  DMA2(lW, 73728, voffW0, srdW, sW, 663552);
  DMA2(lW, 74752, voffW0, srdW, sW, 691200);
  DMA2(lW, 75776, voffW0, srdW, sW, 718848);
  SB; asm volatile("s_bitcmp1_b32 48, %[cr]\n\ts_cbranch_scc0 1f\n\ts_waitcnt vmcnt(6)\n1:\n\ts_bitcmp1_b32 128, %[cr]\n\ts_cbranch_scc0 2f\n\ts_waitcnt vmcnt(11)\n2:\n\t" : "+v"(ep), "+v"(pfv), "+v"(ft) : [cr] "s"(w) : "memory", "scc"); SB;
  if (tid == 448) *(LAS u32x2*)(size_t)162816 = ep;
  SB; asm volatile("s_bitcmp1_b32 79, %[cr]\n\ts_cbranch_scc0 1f\n\ts_waitcnt vmcnt(11)\n1:\n\t" : "+v"(xr[2][0]), "+v"(xr[2][1]), "+v"(xr[2][2]), "+v"(xr[2][3]), "+v"(xt[2]) : [cr] "s"(w) : "memory", "scc"); SB;
  float rl2, lc2; ln_stats(xr[2], xt[2], rl2, lc2); SB;
  SB; asm volatile("s_waitcnt lgkmcnt(0)\n\ts_barrier" :  : [cr] "s"(w) : "memory", "scc"); SB;
  if (rg == 1) {
    const u32 xa0 = OPQ(ldsXf0 + 0), xa1 = OPQ(ldsXf1 + 0), xa2 = OPQ(ldsXf2 + 0), xa3 = OPQ(ldsXf3 + 0);
    xf[0] = *(LAS s16x8*)(size_t)(xa0 + 0);
    xf[1] = *(LAS s16x8*)(size_t)(xa1 + 0);
    xf[2] = *(LAS s16x8*)(size_t)(xa2 + 0);
    xf[3] = *(LAS s16x8*)(size_t)(xa3 + 0);
    xf[4] = *(LAS s16x8*)(size_t)(xa0 + 128);
    xf[5] = *(LAS s16x8*)(size_t)(xa1 + 128);
    xf[6] = *(LAS s16x8*)(size_t)(xa2 + 128);
    SB; ln_apply<0, 1>(xr[2], xt[2], rl2, lc2, ldsLnW0, ldsLnT0); SB;
    xf[7] = *(LAS s16x8*)(size_t)(xa3 + 128);
    xf[8] = *(LAS s16x8*)(size_t)(xa0 + 256);
    xf[9] = *(LAS s16x8*)(size_t)(xa1 + 256);
    xf[10] = *(LAS s16x8*)(size_t)(xa2 + 256);
    xf[11] = *(LAS s16x8*)(size_t)(xa3 + 256);
    xf[12] = *(LAS s16x8*)(size_t)(xa0 + 384);
    xf[13] = *(LAS s16x8*)(size_t)(xa1 + 384);
    SB; ln_apply<1, 2>(xr[2], xt[2], rl2, lc2, ldsLnW0, ldsLnT0); SB;
    xf[14] = *(LAS s16x8*)(size_t)(xa2 + 384);
    xf[15] = *(LAS s16x8*)(size_t)(xa3 + 384);
    xf[16] = *(LAS s16x8*)(size_t)(xa0 + 512);
    xf[17] = *(LAS s16x8*)(size_t)(xa1 + 512);
    xf[18] = *(LAS s16x8*)(size_t)(xa2 + 512);
    xf[19] = *(LAS s16x8*)(size_t)(xa3 + 512);
    xf[20] = *(LAS s16x8*)(size_t)(xa0 + 640);
    SB; ln_apply<2, 3>(xr[2], xt[2], rl2, lc2, ldsLnW0, ldsLnT0); SB;
    xf[21] = *(LAS s16x8*)(size_t)(xa1 + 640);
    xf[22] = *(LAS s16x8*)(size_t)(xa2 + 640);
    xf[23] = *(LAS s16x8*)(size_t)(xa3 + 640);
    xf[24] = *(LAS s16x8*)(size_t)(xa0 + 768);
    xf[25] = *(LAS s16x8*)(size_t)(xa1 + 768);
    xf[26] = *(LAS s16x8*)(size_t)(xa2 + 768);
    xf[27] = *(LAS s16x8*)(size_t)(xa3 + 768);
    SB; ln_apply<3, 4>(xr[2], xt[2], rl2, lc2, ldsLnW0, ldsLnT0); SB;
    xf[28] = *(LAS s16x8*)(size_t)(xa0 + 896);
    xf[29] = *(LAS s16x8*)(size_t)(xa1 + 896);
    xf[30] = *(LAS s16x8*)(size_t)(xa2 + 896);
    xf[31] = *(LAS s16x8*)(size_t)(xa3 + 896);
    xf[32] = *(LAS s16x8*)(size_t)(xa0 + 1024);
    xf[33] = *(LAS s16x8*)(size_t)(xa1 + 1024);
    xf[34] = *(LAS s16x8*)(size_t)(xa2 + 1024);
    xf[35] = *(LAS s16x8*)(size_t)(xa3 + 1024);
    SB; ln_apply<4, 5>(xr[2], xt[2], rl2, lc2, ldsLnW0, ldsLnT0); SB;
  } else { ln_apply<0, 5>(xr[2], xt[2], rl2, lc2, ldsLnW0, ldsLnT0); SB; }
  DMA2(lW, 98304, voffW0, srdW, sW, 1327104);
  DMA2(lW, 99328, voffW0, srdW, sW, 1354752);
  DMA2(lW, 100352, voffW0, srdW, sW, 1382400);
  SB; asm volatile("s_bitcmp1_b32 207, %[cr]\n\ts_cbranch_scc0 1f\n\ts_waitcnt vmcnt(9)\n1:\n\t" : "+v"(xr[3][0]), "+v"(xr[3][1]), "+v"(xr[3][2]), "+v"(xr[3][3]), "+v"(xt[3]) : [cr] "s"(w) : "memory", "scc"); SB;
  float rl3, lc3; ln_stats(xr[3], xt[3], rl3, lc3); SB;
  SB; asm volatile("s_waitcnt lgkmcnt(0)\n\ts_barrier" :  : [cr] "s"(w) : "memory", "scc"); SB;
  if (rg == 2) {
    const u32 xa0 = OPQ(ldsXf0 + 122880), xa1 = OPQ(ldsXf1 + 122880), xa2 = OPQ(ldsXf2 + 122880), xa3 = OPQ(ldsXf3 + 122880);
    xf[0] = *(LAS s16x8*)(size_t)(xa0 + 0);
    xf[1] = *(LAS s16x8*)(size_t)(xa1 + 0);
    xf[2] = *(LAS s16x8*)(size_t)(xa2 + 0);
    xf[3] = *(LAS s16x8*)(size_t)(xa3 + 0);
    xf[4] = *(LAS s16x8*)(size_t)(xa0 + 128);
    xf[5] = *(LAS s16x8*)(size_t)(xa1 + 128);
    xf[6] = *(LAS s16x8*)(size_t)(xa2 + 128);
    SB; ln_apply<0, 1>(xr[3], xt[3], rl3, lc3, ldsLnW1, ldsLnT1); SB;
    xf[7] = *(LAS s16x8*)(size_t)(xa3 + 128);
    xf[8] = *(LAS s16x8*)(size_t)(xa0 + 256);
    xf[9] = *(LAS s16x8*)(size_t)(xa1 + 256);
    xf[10] = *(LAS s16x8*)(size_t)(xa2 + 256);
    xf[11] = *(LAS s16x8*)(size_t)(xa3 + 256);
    xf[12] = *(LAS s16x8*)(size_t)(xa0 + 384);
    xf[13] = *(LAS s16x8*)(size_t)(xa1 + 384);
    SB; ln_apply<1, 2>(xr[3], xt[3], rl3, lc3, ldsLnW1, ldsLnT1); SB;
    xf[14] = *(LAS s16x8*)(size_t)(xa2 + 384);
    xf[15] = *(LAS s16x8*)(size_t)(xa3 + 384);
    xf[16] = *(LAS s16x8*)(size_t)(xa0 + 512);
    xf[17] = *(LAS s16x8*)(size_t)(xa1 + 512);
    xf[18] = *(LAS s16x8*)(size_t)(xa2 + 512);
    xf[19] = *(LAS s16x8*)(size_t)(xa3 + 512);
    xf[20] = *(LAS s16x8*)(size_t)(xa0 + 640);
    SB; ln_apply<2, 3>(xr[3], xt[3], rl3, lc3, ldsLnW1, ldsLnT1); SB;
    xf[21] = *(LAS s16x8*)(size_t)(xa1 + 640);
    xf[22] = *(LAS s16x8*)(size_t)(xa2 + 640);
    xf[23] = *(LAS s16x8*)(size_t)(xa3 + 640);
    xf[24] = *(LAS s16x8*)(size_t)(xa0 + 768);
    xf[25] = *(LAS s16x8*)(size_t)(xa1 + 768);
    xf[26] = *(LAS s16x8*)(size_t)(xa2 + 768);
    xf[27] = *(LAS s16x8*)(size_t)(xa3 + 768);
    SB; ln_apply<3, 4>(xr[3], xt[3], rl3, lc3, ldsLnW1, ldsLnT1); SB;
    xf[28] = *(LAS s16x8*)(size_t)(xa0 + 896);
    xf[29] = *(LAS s16x8*)(size_t)(xa1 + 896);
    xf[30] = *(LAS s16x8*)(size_t)(xa2 + 896);
    xf[31] = *(LAS s16x8*)(size_t)(xa3 + 896);
    xf[32] = *(LAS s16x8*)(size_t)(xa0 + 1024);
    xf[33] = *(LAS s16x8*)(size_t)(xa1 + 1024);
    xf[34] = *(LAS s16x8*)(size_t)(xa2 + 1024);
    xf[35] = *(LAS s16x8*)(size_t)(xa3 + 1024);
    SB; ln_apply<4, 5>(xr[3], xt[3], rl3, lc3, ldsLnW1, ldsLnT1); SB;
  } else { ln_apply<0, 5>(xr[3], xt[3], rl3, lc3, ldsLnW1, ldsLnT1); SB; }
  SB; asm volatile("s_bitcmp1_b32 255, %[cr]\n\ts_cbranch_scc0 1f\n\ts_waitcnt vmcnt(0)\n1:\n\ts_waitcnt lgkmcnt(0)\n\ts_barrier" :  : [cr] "s"(w) : "memory", "scc"); SB;
  if (rg == 3) {
    const u32 xa0 = OPQ(ldsXf0 + 0), xa1 = OPQ(ldsXf1 + 0), xa2 = OPQ(ldsXf2 + 0), xa3 = OPQ(ldsXf3 + 0);
    xf[0] = *(LAS s16x8*)(size_t)(xa0 + 0);
    xf[1] = *(LAS s16x8*)(size_t)(xa1 + 0);
    xf[2] = *(LAS s16x8*)(size_t)(xa2 + 0);
    xf[3] = *(LAS s16x8*)(size_t)(xa3 + 0);
    xf[4] = *(LAS s16x8*)(size_t)(xa0 + 128);
    xf[5] = *(LAS s16x8*)(size_t)(xa1 + 128);
    xf[6] = *(LAS s16x8*)(size_t)(xa2 + 128);
    xf[7] = *(LAS s16x8*)(size_t)(xa3 + 128);
    xf[8] = *(LAS s16x8*)(size_t)(xa0 + 256);
    xf[9] = *(LAS s16x8*)(size_t)(xa1 + 256);
    xf[10] = *(LAS s16x8*)(size_t)(xa2 + 256);
    xf[11] = *(LAS s16x8*)(size_t)(xa3 + 256);
    xf[12] = *(LAS s16x8*)(size_t)(xa0 + 384);
    xf[13] = *(LAS s16x8*)(size_t)(xa1 + 384);
    xf[14] = *(LAS s16x8*)(size_t)(xa2 + 384);
    xf[15] = *(LAS s16x8*)(size_t)(xa3 + 384);
    xf[16] = *(LAS s16x8*)(size_t)(xa0 + 512);
    xf[17] = *(LAS s16x8*)(size_t)(xa1 + 512);
    xf[18] = *(LAS s16x8*)(size_t)(xa2 + 512);
    xf[19] = *(LAS s16x8*)(size_t)(xa3 + 512);
    xf[20] = *(LAS s16x8*)(size_t)(xa0 + 640);
    xf[21] = *(LAS s16x8*)(size_t)(xa1 + 640);
    xf[22] = *(LAS s16x8*)(size_t)(xa2 + 640);
    xf[23] = *(LAS s16x8*)(size_t)(xa3 + 640);
    xf[24] = *(LAS s16x8*)(size_t)(xa0 + 768);
    xf[25] = *(LAS s16x8*)(size_t)(xa1 + 768);
    xf[26] = *(LAS s16x8*)(size_t)(xa2 + 768);
    xf[27] = *(LAS s16x8*)(size_t)(xa3 + 768);
    xf[28] = *(LAS s16x8*)(size_t)(xa0 + 896);
    xf[29] = *(LAS s16x8*)(size_t)(xa1 + 896);
    xf[30] = *(LAS s16x8*)(size_t)(xa2 + 896);
    xf[31] = *(LAS s16x8*)(size_t)(xa3 + 896);
    xf[32] = *(LAS s16x8*)(size_t)(xa0 + 1024);
    xf[33] = *(LAS s16x8*)(size_t)(xa1 + 1024);
    xf[34] = *(LAS s16x8*)(size_t)(xa2 + 1024);
    xf[35] = *(LAS s16x8*)(size_t)(xa3 + 1024);
  }
  ep = *(LAS u32x2*)(size_t)162816;
  const u64 E0 = ((u64)(u32)__builtin_amdgcn_readfirstlane(ep[1]) << 32) | (u32)__builtin_amdgcn_readfirstlane(ep[0]);
  const u64 TAG = (E0 * 6364136223846793005ull + 0x9E3779B97F4A7C1Dull) & ~7ull;
  const u32 tagLo = (u32)TAG, tagHi = (u32)(TAG >> 32);
  f32x16 A0, A1; u32x4 hp0, hp1; const u32 vl16 = ln * 16;
  const s16x8 w0 = TR2(ldsT1, 0, 512);
  const s16x8 w1 = TR2(ldsT1, 2048, 2560);
  const s16x8 w2 = TR2(ldsT1, 4096, 4608);
  const u32 voffW1 = qW0 + 1408 + (lcA >= 2 ? (u32)(864) : 0u);
  A0 = MF32(xf[0], w0, zero16);
  SB;
  const s16x8 w3 = TR2(ldsT1, 6144, 6656);
  asm volatile("" :: "v"(w0));
  SB;
  A0 = MF32(xf[1], w1, A0);
  SB;
  const s16x8 w4 = TR2(ldsT1, 8192, 8704);
  asm volatile("" :: "v"(w1));
  SB;
  A0 = MF32(xf[2], w2, A0);
  SB;
  const s16x8 w5 = TR2(ldsT1, 10240, 10752);
  asm volatile("" :: "v"(w2));
  DMA2(lW, 122880, voffW1, srdW, sW, 0);
  SB;
  A0 = MF32(xf[3], w3, A0);
  SB;
  const s16x8 w6 = TR2(ldsT1, 12288, 12800);
  asm volatile("" :: "v"(w3));
  SB;
  A0 = MF32(xf[4], w4, A0);
  SB;
  const s16x8 w7 = TR2(ldsT1, 14336, 14848);
  asm volatile("" :: "v"(w4));
  SB;
  A0 = MF32(xf[5], w5, A0);
  SB;
  const s16x8 w8 = TR2(ldsT1, 16384, 16896);
  asm volatile("" :: "v"(w5));
  SB;
  A0 = MF32(xf[6], w6, A0);
  SB;
  const s16x8 w9 = TR2(ldsT1, 18432, 18944);
  asm volatile("" :: "v"(w6));
  DMA2(lW, 123904, voffW1, srdW, sW, 27648);
  SB;
  A0 = MF32(xf[7], w7, A0);
  SB;
  const s16x8 w10 = TR2(ldsT1, 20480, 20992);
  asm volatile("" :: "v"(w7));
  SB;
  A0 = MF32(xf[8], w8, A0);
  SB;
  const s16x8 w11 = TR2(ldsT1, 22528, 23040);
  asm volatile("" :: "v"(w8));
  SB;
  A0 = MF32(xf[9], w9, A0);
  SB;
  const s16x8 w12 = TR2(ldsT1, 24576, 25088);
  asm volatile("" :: "v"(w9));
  SB;
  A0 = MF32(xf[10], w10, A0);
  SB;
  const s16x8 w13 = TR2(ldsT1, 26624, 27136);
  asm volatile("" :: "v"(w10));
  DMA2(lW, 124928, voffW1, srdW, sW, 55296);
  SB;
  A0 = MF32(xf[11], w11, A0);
  SB;
  const s16x8 w14 = TR2(ldsT1, 28672, 29184);
  asm volatile("" :: "v"(w11));
  SB;
  SB; asm volatile("s_waitcnt lgkmcnt(0)\n\ts_barrier" :  :: "memory"); SB;
  A0 = MF32(xf[12], w12, A0);
  SB;
  const s16x8 w15 = TR2(ldsT1, 30720, 31232);
  asm volatile("" :: "v"(w12));
  SB;
  A0 = MF32(xf[13], w13, A0);
  SB;
  const s16x8 w16 = TR2(ldsT1, 32768, 33280);
  asm volatile("" :: "v"(w13));
  DMA2(lW, 24576, voffW1, srdW, sW, 663552);
  SB;
  A0 = MF32(xf[14], w14, A0);
  SB;
  const s16x8 w17 = TR2(ldsT1, 34816, 35328);
  asm volatile("" :: "v"(w14));
  SB;
  A0 = MF32(xf[15], w15, A0);
  SB;
  const s16x8 w18 = TR2(ldsT1, 36864, 37376);
  asm volatile("" :: "v"(w15));
  DMA2(lW, 25600, voffW1, srdW, sW, 691200);
  SB;
  A0 = MF32(xf[16], w16, A0);
  SB;
  const s16x8 w19 = TR2(ldsT1, 38912, 39424);
  asm volatile("" :: "v"(w16));
  SB;
  A0 = MF32(xf[17], w17, A0);
  SB;
  const s16x8 w20 = TR2(ldsT1, 40960, 41472);
  asm volatile("" :: "v"(w17));
  DMA2(lW, 26624, voffW1, srdW, sW, 718848);
  SB;
  A0 = MF32(xf[18], w18, A0);
  SB;
  const s16x8 w21 = TR2(ldsT1, 43008, 43520);
  asm volatile("" :: "v"(w18));
  SB;
  A0 = MF32(xf[19], w19, A0);
  SB;
  const s16x8 w22 = TR2(ldsT1, 45056, 45568);
  asm volatile("" :: "v"(w19));
  DMA2(lW, 49152, voffW1, srdW, sW, 1327104);
  SB;
  A0 = MF32(xf[20], w20, A0);
  SB;
  const s16x8 w23 = TR2(ldsT1, 47104, 47616);
  asm volatile("" :: "v"(w20));
  SB;
  A0 = MF32(xf[21], w21, A0);
  SB;
  const s16x8 w24 = TR2(ldsT2, 0, 512);
  asm volatile("" :: "v"(w21));
  DMA2(lW, 50176, voffW1, srdW, sW, 1354752);
  SB;
  A0 = MF32(xf[22], w22, A0);
  SB;
  const s16x8 w25 = TR2(ldsT2, 2048, 2560);
  asm volatile("" :: "v"(w22));
  SB;
  A0 = MF32(xf[23], w23, A0);
  SB;
  const s16x8 w26 = TR2(ldsT2, 4096, 4608);
  asm volatile("" :: "v"(w23));
  DMA2(lW, 51200, voffW1, srdW, sW, 1382400);
  SB;
  SB; asm volatile("s_waitcnt vmcnt(6)\n\ts_barrier" :  :: "memory"); SB;
  const u32 voffW2 = qW0 + 2400;
  A0 = MF32(xf[24], w24, A0);
  SB;
  const s16x8 w27 = TR2(ldsT2, 6144, 6656);
  asm volatile("" :: "v"(w24));
  SB;
  A0 = MF32(xf[25], w25, A0);
  SB;
  const s16x8 w28 = TR2(ldsT2, 8192, 8704);
  asm volatile("" :: "v"(w25));
  SB;
  A0 = MF32(xf[26], w26, A0);
  SB;
  const s16x8 w29 = TR2(ldsT2, 10240, 10752);
  asm volatile("" :: "v"(w26));
  DMA2(lW, 73728, voffW2, srdW, sW, 0);
  SB;
  A0 = MF32(xf[27], w27, A0);
  SB;
  const s16x8 w30 = TR2(ldsT2, 12288, 12800);
  asm volatile("" :: "v"(w27));
  SB;
  A0 = MF32(xf[28], w28, A0);
  SB;
  const s16x8 w31 = TR2(ldsT2, 14336, 14848);
  asm volatile("" :: "v"(w28));
  SB;
  A0 = MF32(xf[29], w29, A0);
  SB;
  const s16x8 w32 = TR2(ldsT2, 16384, 16896);
  asm volatile("" :: "v"(w29));
  SB;
  A0 = MF32(xf[30], w30, A0);
  SB;
  const s16x8 w33 = TR2(ldsT2, 18432, 18944);
  asm volatile("" :: "v"(w30));
  DMA2(lW, 74752, voffW2, srdW, sW, 27648);
  SB;
  A0 = MF32(xf[31], w31, A0);
  SB;
  const s16x8 w34 = TR2(ldsT2, 20480, 20992);
  asm volatile("" :: "v"(w31));
  SB;
  A0 = MF32(xf[32], w32, A0);
  SB;
  const s16x8 w35 = TR2(ldsT2, 22528, 23040);
  asm volatile("" :: "v"(w32));
  SB;
  A0 = MF32(xf[33], w33, A0);
  SB;
  const s16x8 w36 = TR2(ldsT2, 24576, 25088);
  asm volatile("" :: "v"(w33));
  SB;
  A0 = MF32(xf[34], w34, A0);
  SB;
  const s16x8 w37 = TR2(ldsT2, 26624, 27136);
  asm volatile("" :: "v"(w34));
  DMA2(lW, 75776, voffW2, srdW, sW, 55296);
  SB;
  A0 = MF32(xf[35], w35, A0);
  SB;
  const s16x8 w38 = TR2(ldsT2, 28672, 29184);
  asm volatile("" :: "v"(w35));
  SB;
  SB; asm volatile("s_waitcnt vmcnt(6)\n\ts_barrier" :  :: "memory"); SB;
  A1 = MF32(xf[0], w36, zero16);
  SB;
  const s16x8 w39 = TR2(ldsT2, 30720, 31232);
  asm volatile("" :: "v"(w36));
  SB;
  A1 = MF32(xf[1], w37, A1);
  SB;
  const s16x8 w40 = TR2(ldsT2, 32768, 33280);
  asm volatile("" :: "v"(w37));
  const u32 tw0 = OPQ((cg ? 139264 : 131072) + rg * 2048 + vl16);
  float e0_0am = A0[0] * LOG2E;
  float e0_0bm = A0[1] * LOG2E;
  e0_0am = __builtin_fminf(e0_0am, 0.0f);
  e0_0bm = __builtin_fminf(e0_0bm, 0.0f);
  SB;
  A1 = MF32(xf[2], w38, A1);
  SB;
  const s16x8 w41 = TR2(ldsT2, 34816, 35328);
  asm volatile("" :: "v"(w38));
  float e0_0ap = A0[0] + 1e-6f;
  float e0_0bp = A0[1] + 1e-6f;
  e0_0am = __builtin_amdgcn_exp2f(e0_0am);
  DMA2(lW, 98304, voffW2, srdW, sW, 663552);
  SB;
  A1 = MF32(xf[3], w39, A1);
  SB;
  const s16x8 w42 = TR2(ldsT2, 36864, 37376);
  asm volatile("" :: "v"(w39));
  e0_0bm = __builtin_amdgcn_exp2f(e0_0bm);
  e0_0ap = __builtin_fmaxf(e0_0ap, 1e-6f);
  e0_0bp = __builtin_fmaxf(e0_0bp, 1e-6f);
  SB;
  A1 = MF32(xf[4], w40, A1);
  SB;
  const s16x8 w43 = TR2(ldsT2, 38912, 39424);
  asm volatile("" :: "v"(w40));
  e0_0ap = e0_0ap + e0_0am;
  e0_0bp = e0_0bp + e0_0bm;
  float zs0 = e0_0ap + e0_0bp;
  hp0[0] = cvtpk(e0_0ap, e0_0bp);
  SB;
  A1 = MF32(xf[5], w41, A1);
  SB;
  const s16x8 w44 = TR2(ldsT2, 40960, 41472);
  asm volatile("" :: "v"(w41));
  float e0_1am = A0[2] * LOG2E;
  float e0_1bm = A0[3] * LOG2E;
  e0_1am = __builtin_fminf(e0_1am, 0.0f);
  e0_1bm = __builtin_fminf(e0_1bm, 0.0f);
  SB;
  A1 = MF32(xf[6], w42, A1);
  SB;
  const s16x8 w45 = TR2(ldsT2, 43008, 43520);
  asm volatile("" :: "v"(w42));
  float e0_1ap = A0[2] + 1e-6f;
  float e0_1bp = A0[3] + 1e-6f;
  e0_1am = __builtin_amdgcn_exp2f(e0_1am);
  DMA2(lW, 99328, voffW2, srdW, sW, 691200);
  SB;
  A1 = MF32(xf[7], w43, A1);
  SB;
  const s16x8 w46 = TR2(ldsT2, 45056, 45568);
  asm volatile("" :: "v"(w43));
  e0_1bm = __builtin_amdgcn_exp2f(e0_1bm);
  e0_1ap = __builtin_fmaxf(e0_1ap, 1e-6f);
  e0_1bp = __builtin_fmaxf(e0_1bp, 1e-6f);
  SB;
  A1 = MF32(xf[8], w44, A1);
  SB;
  const s16x8 w47 = TR2(ldsT2, 47104, 47616);
  asm volatile("" :: "v"(w44));
  e0_1ap = e0_1ap + e0_1am;
  e0_1bp = e0_1bp + e0_1bm;
  zs0 += e0_1ap;
  zs0 += e0_1bp;
  hp0[1] = cvtpk(e0_1ap, e0_1bp);
  SB;
  A1 = MF32(xf[9], w45, A1);
  SB;
  const s16x8 w48 = TR2(ldsT0, 24576, 25088);
  asm volatile("" :: "v"(w45));
  float e0_2am = A0[4] * LOG2E;
  float e0_2bm = A0[5] * LOG2E;
  e0_2am = __builtin_fminf(e0_2am, 0.0f);
  e0_2bm = __builtin_fminf(e0_2bm, 0.0f);
  SB;
  A1 = MF32(xf[10], w46, A1);
  SB;
  const s16x8 w49 = TR2(ldsT0, 26624, 27136);
  asm volatile("" :: "v"(w46));
  float e0_2ap = A0[4] + 1e-6f;
  float e0_2bp = A0[5] + 1e-6f;
  e0_2am = __builtin_amdgcn_exp2f(e0_2am);
  DMA2(lW, 100352, voffW2, srdW, sW, 718848);
  SB;
  A1 = MF32(xf[11], w47, A1);
  SB;
  const s16x8 w50 = TR2(ldsT0, 28672, 29184);
  asm volatile("" :: "v"(w47));
  e0_2bm = __builtin_amdgcn_exp2f(e0_2bm);
  e0_2ap = __builtin_fmaxf(e0_2ap, 1e-6f);
  e0_2bp = __builtin_fmaxf(e0_2bp, 1e-6f);
  SB;
  SB; asm volatile("s_waitcnt vmcnt(6)\n\ts_barrier" :  :: "memory"); SB;
  A1 = MF32(xf[12], w48, A1);
  SB;
  const s16x8 w51 = TR2(ldsT0, 30720, 31232);
  asm volatile("" :: "v"(w48));
  e0_2ap = e0_2ap + e0_2am;
  e0_2bp = e0_2bp + e0_2bm;
  zs0 += e0_2ap;
  zs0 += e0_2bp;
  SB;
  A1 = MF32(xf[13], w49, A1);
  SB;
  const s16x8 w52 = TR2(ldsT0, 32768, 33280);
  asm volatile("" :: "v"(w49));
  hp0[2] = cvtpk(e0_2ap, e0_2bp);
  float e0_3am = A0[6] * LOG2E;
  float e0_3bm = A0[7] * LOG2E;
  e0_3am = __builtin_fminf(e0_3am, 0.0f);
  SB;
  A1 = MF32(xf[14], w50, A1);
  SB;
  const s16x8 w53 = TR2(ldsT0, 34816, 35328);
  asm volatile("" :: "v"(w50));
  e0_3bm = __builtin_fminf(e0_3bm, 0.0f);
  float e0_3ap = A0[6] + 1e-6f;
  float e0_3bp = A0[7] + 1e-6f;
  e0_3am = __builtin_amdgcn_exp2f(e0_3am);
  SB;
  A1 = MF32(xf[15], w51, A1);
  SB;
  const s16x8 w54 = TR2(ldsT0, 36864, 37376);
  asm volatile("" :: "v"(w51));
  e0_3bm = __builtin_amdgcn_exp2f(e0_3bm);
  e0_3ap = __builtin_fmaxf(e0_3ap, 1e-6f);
  SB;
  A1 = MF32(xf[16], w52, A1);
  SB;
  const s16x8 w55 = TR2(ldsT0, 38912, 39424);
  asm volatile("" :: "v"(w52));
  e0_3bp = __builtin_fmaxf(e0_3bp, 1e-6f);
  e0_3ap = e0_3ap + e0_3am;
  e0_3bp = e0_3bp + e0_3bm;
  zs0 += e0_3ap;
  zs0 += e0_3bp;
  SB;
  A1 = MF32(xf[17], w53, A1);
  SB;
  const s16x8 w56 = TR2(ldsT0, 40960, 41472);
  asm volatile("" :: "v"(w53));
  hp0[3] = cvtpk(e0_3ap, e0_3bp);
  float e0_4am = A0[8] * LOG2E;
  float e0_4bm = A0[9] * LOG2E;
  SB;
  A1 = MF32(xf[18], w54, A1);
  SB;
  const s16x8 w57 = TR2(ldsT0, 43008, 43520);
  asm volatile("" :: "v"(w54));
  *(LAS u32x4*)(size_t)(tw0 + 0) = hp0;
  e0_4am = __builtin_fminf(e0_4am, 0.0f);
  e0_4bm = __builtin_fminf(e0_4bm, 0.0f);
  float e0_4ap = A0[8] + 1e-6f;
  float e0_4bp = A0[9] + 1e-6f;
  SB;
  A1 = MF32(xf[19], w55, A1);
  SB;
  const s16x8 w58 = TR2(ldsT0, 45056, 45568);
  asm volatile("" :: "v"(w55));
  e0_4am = __builtin_amdgcn_exp2f(e0_4am);
  e0_4bm = __builtin_amdgcn_exp2f(e0_4bm);
  SB;
  A1 = MF32(xf[20], w56, A1);
  SB;
  const s16x8 w59 = TR2(ldsT0, 47104, 47616);
  asm volatile("" :: "v"(w56));
  e0_4ap = __builtin_fmaxf(e0_4ap, 1e-6f);
  e0_4bp = __builtin_fmaxf(e0_4bp, 1e-6f);
  e0_4ap = e0_4ap + e0_4am;
  e0_4bp = e0_4bp + e0_4bm;
  SB;
  A1 = MF32(xf[21], w57, A1);
  SB;
  const s16x8 w60 = TR2(ldsT1, 0, 512);
  asm volatile("" :: "v"(w57));
  zs0 += e0_4ap;
  zs0 += e0_4bp;
  hp1[0] = cvtpk(e0_4ap, e0_4bp);
  float e0_5am = A0[10] * LOG2E;
  SB;
  A1 = MF32(xf[22], w58, A1);
  SB;
  const s16x8 w61 = TR2(ldsT1, 2048, 2560);
  asm volatile("" :: "v"(w58));
  float e0_5bm = A0[11] * LOG2E;
  e0_5am = __builtin_fminf(e0_5am, 0.0f);
  e0_5bm = __builtin_fminf(e0_5bm, 0.0f);
  float e0_5ap = A0[10] + 1e-6f;
  SB;
  A1 = MF32(xf[23], w59, A1);
  SB;
  const s16x8 w62 = TR2(ldsT1, 4096, 4608);
  asm volatile("" :: "v"(w59));
  float e0_5bp = A0[11] + 1e-6f;
  e0_5am = __builtin_amdgcn_exp2f(e0_5am);
  e0_5bm = __builtin_amdgcn_exp2f(e0_5bm);
  SB;
  SB; asm volatile("s_waitcnt vmcnt(3)\n\ts_barrier" :  :: "memory"); SB;
  A1 = MF32(xf[24], w60, A1);
  SB;
  const s16x8 w63 = TR2(ldsT1, 6144, 6656);
  asm volatile("" :: "v"(w60));
  e0_5ap = __builtin_fmaxf(e0_5ap, 1e-6f);
  e0_5bp = __builtin_fmaxf(e0_5bp, 1e-6f);
  e0_5ap = e0_5ap + e0_5am;
  e0_5bp = e0_5bp + e0_5bm;
  SB;
  A1 = MF32(xf[25], w61, A1);
  SB;
  const s16x8 w64 = TR2(ldsT1, 8192, 8704);
  asm volatile("" :: "v"(w61));
  zs0 += e0_5ap;
  zs0 += e0_5bp;
  hp1[1] = cvtpk(e0_5ap, e0_5bp);
  float e0_6am = A0[12] * LOG2E;
  SB;
  A1 = MF32(xf[26], w62, A1);
  SB;
  const s16x8 w65 = TR2(ldsT1, 10240, 10752);
  asm volatile("" :: "v"(w62));
  float e0_6bm = A0[13] * LOG2E;
  e0_6am = __builtin_fminf(e0_6am, 0.0f);
  e0_6bm = __builtin_fminf(e0_6bm, 0.0f);
  float e0_6ap = A0[12] + 1e-6f;
  DMA2(lW, 24576, voffW2, srdW, sW, 1327104);
  SB;
  A1 = MF32(xf[27], w63, A1);
  SB;
  const s16x8 w66 = TR2(ldsT1, 12288, 12800);
  asm volatile("" :: "v"(w63));
  float e0_6bp = A0[13] + 1e-6f;
  e0_6am = __builtin_amdgcn_exp2f(e0_6am);
  e0_6bm = __builtin_amdgcn_exp2f(e0_6bm);
  SB;
  A1 = MF32(xf[28], w64, A1);
  SB;
  const s16x8 w67 = TR2(ldsT1, 14336, 14848);
  asm volatile("" :: "v"(w64));
  e0_6ap = __builtin_fmaxf(e0_6ap, 1e-6f);
  e0_6bp = __builtin_fmaxf(e0_6bp, 1e-6f);
  e0_6ap = e0_6ap + e0_6am;
  SB;
  A1 = MF32(xf[29], w65, A1);
  SB;
  const s16x8 w68 = TR2(ldsT1, 16384, 16896);
  asm volatile("" :: "v"(w65));
  e0_6bp = e0_6bp + e0_6bm;
  zs0 += e0_6ap;
  zs0 += e0_6bp;
  hp1[2] = cvtpk(e0_6ap, e0_6bp);
  SB;
  A1 = MF32(xf[30], w66, A1);
  SB;
  const s16x8 w69 = TR2(ldsT1, 18432, 18944);
  asm volatile("" :: "v"(w66));
  float e0_7am = A0[14] * LOG2E;
  float e0_7bm = A0[15] * LOG2E;
  e0_7am = __builtin_fminf(e0_7am, 0.0f);
  e0_7bm = __builtin_fminf(e0_7bm, 0.0f);
  DMA2(lW, 25600, voffW2, srdW, sW, 1354752);
  SB;
  A1 = MF32(xf[31], w67, A1);
  SB;
  const s16x8 w70 = TR2(ldsT1, 20480, 20992);
  asm volatile("" :: "v"(w67));
  float e0_7ap = A0[14] + 1e-6f;
  float e0_7bp = A0[15] + 1e-6f;
  e0_7am = __builtin_amdgcn_exp2f(e0_7am);
  SB;
  A1 = MF32(xf[32], w68, A1);
  SB;
  const s16x8 w71 = TR2(ldsT1, 22528, 23040);
  asm volatile("" :: "v"(w68));
  e0_7bm = __builtin_amdgcn_exp2f(e0_7bm);
  e0_7ap = __builtin_fmaxf(e0_7ap, 1e-6f);
  e0_7bp = __builtin_fmaxf(e0_7bp, 1e-6f);
  e0_7ap = e0_7ap + e0_7am;
  SB;
  A1 = MF32(xf[33], w69, A1);
  SB;
  const s16x8 w72 = TR2(ldsT1, 24576, 25088);
  asm volatile("" :: "v"(w69));
  e0_7bp = e0_7bp + e0_7bm;
  zs0 += e0_7ap;
  zs0 += e0_7bp;
  hp1[3] = cvtpk(e0_7ap, e0_7bp);
  SB;
  A1 = MF32(xf[34], w70, A1);
  SB;
  const s16x8 w73 = TR2(ldsT1, 26624, 27136);
  asm volatile("" :: "v"(w70));
  { const auto zp = __builtin_amdgcn_permlane32_swap(__float_as_uint(zs0), __float_as_uint(zs0), false, false); *(LAS float*)(size_t)(16384 + rg * 768 + (0 + cg) * 128 + t31 * 4) = __uint_as_float(zp[0]) + __uint_as_float(zp[1]); }
  DMA2(lW, 26624, voffW2, srdW, sW, 1382400);
  SB;
  A1 = MF32(xf[35], w71, A1);
  SB;
  const s16x8 w74 = TR2(ldsT1, 28672, 29184);
  asm volatile("" :: "v"(w71));
  *(LAS u32x4*)(size_t)(tw0 + 1024) = hp1;
  SB;
  SB; asm volatile("s_waitcnt vmcnt(3)\n\ts_barrier" :  :: "memory"); SB;
  const u32 voffW3 = qW0 + 1280;
  A0 = MF32(xf[0], w72, zero16);
  SB;
  const s16x8 w75 = TR2(ldsT1, 30720, 31232);
  asm volatile("" :: "v"(w72));
  SB;
  A0 = MF32(xf[1], w73, A0);
  SB;
  const s16x8 w76 = TR2(ldsT1, 32768, 33280);
  asm volatile("" :: "v"(w73));
  const u32 tw1 = OPQ((cg ? 147456 : 0) + rg * 2048 + vl16);
  const bool pm1 = cg ? false : (t31 < 16);
  float e1_0am = A1[0] * LOG2E;
  float e1_0bm = A1[1] * LOG2E;
  e1_0am = __builtin_fminf(e1_0am, 0.0f);
  SB;
  A0 = MF32(xf[2], w74, A0);
  SB;
  const s16x8 w77 = TR2(ldsT1, 34816, 35328);
  asm volatile("" :: "v"(w74));
  e1_0bm = __builtin_fminf(e1_0bm, 0.0f);
  float e1_0ap = A1[0] + 1e-6f;
  float e1_0bp = A1[1] + 1e-6f;
  e1_0am = __builtin_amdgcn_exp2f(e1_0am);
  DMA2(lW, 49152, voffW3, srdW, sW, 0);
  SB;
  A0 = MF32(xf[3], w75, A0);
  SB;
  const s16x8 w78 = TR2(ldsT1, 36864, 37376);
  asm volatile("" :: "v"(w75));
  e1_0bm = __builtin_amdgcn_exp2f(e1_0bm);
  e1_0ap = __builtin_fmaxf(e1_0ap, 1e-6f);
  e1_0bp = __builtin_fmaxf(e1_0bp, 1e-6f);
  SB;
  A0 = MF32(xf[4], w76, A0);
  SB;
  const s16x8 w79 = TR2(ldsT1, 38912, 39424);
  asm volatile("" :: "v"(w76));
  e1_0ap = e1_0ap + e1_0am;
  e1_0bp = e1_0bp + e1_0bm;
  e1_0ap = pm1 ? e1_0ap : A1[0];
  e1_0bp = pm1 ? e1_0bp : A1[1];
  float zs1 = e1_0ap + e1_0bp;
  SB;
  A0 = MF32(xf[5], w77, A0);
  SB;
  const s16x8 w80 = TR2(ldsT1, 40960, 41472);
  asm volatile("" :: "v"(w77));
  hp0[0] = cvtpk(e1_0ap, e1_0bp);
  float e1_1am = A1[2] * LOG2E;
  float e1_1bm = A1[3] * LOG2E;
  e1_1am = __builtin_fminf(e1_1am, 0.0f);
  e1_1bm = __builtin_fminf(e1_1bm, 0.0f);
  SB;
  A0 = MF32(xf[6], w78, A0);
  SB;
  const s16x8 w81 = TR2(ldsT1, 43008, 43520);
  asm volatile("" :: "v"(w78));
  float e1_1ap = A1[2] + 1e-6f;
  float e1_1bp = A1[3] + 1e-6f;
  e1_1am = __builtin_amdgcn_exp2f(e1_1am);
  DMA2(lW, 50176, voffW3, srdW, sW, 27648);
  SB;
  A0 = MF32(xf[7], w79, A0);
  SB;
  const s16x8 w82 = TR2(ldsT1, 45056, 45568);
  asm volatile("" :: "v"(w79));
  e1_1bm = __builtin_amdgcn_exp2f(e1_1bm);
  e1_1ap = __builtin_fmaxf(e1_1ap, 1e-6f);
  e1_1bp = __builtin_fmaxf(e1_1bp, 1e-6f);
  e1_1ap = e1_1ap + e1_1am;
  SB;
  A0 = MF32(xf[8], w80, A0);
  SB;
  const s16x8 w83 = TR2(ldsT1, 47104, 47616);
  asm volatile("" :: "v"(w80));
  e1_1bp = e1_1bp + e1_1bm;
  e1_1ap = pm1 ? e1_1ap : A1[2];
  e1_1bp = pm1 ? e1_1bp : A1[3];
  zs1 += e1_1ap;
  zs1 += e1_1bp;
  SB;
  A0 = MF32(xf[9], w81, A0);
  SB;
  const s16x8 w84 = TR2(ldsT2, 0, 512);
  asm volatile("" :: "v"(w81));
  hp0[1] = cvtpk(e1_1ap, e1_1bp);
  float e1_2am = A1[4] * LOG2E;
  float e1_2bm = A1[5] * LOG2E;
  e1_2am = __builtin_fminf(e1_2am, 0.0f);
  SB;
  A0 = MF32(xf[10], w82, A0);
  SB;
  const s16x8 w85 = TR2(ldsT2, 2048, 2560);
  asm volatile("" :: "v"(w82));
  e1_2bm = __builtin_fminf(e1_2bm, 0.0f);
  float e1_2ap = A1[4] + 1e-6f;
  float e1_2bp = A1[5] + 1e-6f;
  e1_2am = __builtin_amdgcn_exp2f(e1_2am);
  DMA2(lW, 51200, voffW3, srdW, sW, 55296);
  SB;
  A0 = MF32(xf[11], w83, A0);
  SB;
  const s16x8 w86 = TR2(ldsT2, 4096, 4608);
  asm volatile("" :: "v"(w83));
  e1_2bm = __builtin_amdgcn_exp2f(e1_2bm);
  e1_2ap = __builtin_fmaxf(e1_2ap, 1e-6f);
  e1_2bp = __builtin_fmaxf(e1_2bp, 1e-6f);
  SB;
  SB; asm volatile("s_waitcnt vmcnt(3)\n\ts_barrier" :  :: "memory"); SB;
  A0 = MF32(xf[12], w84, A0);
  SB;
  const s16x8 w87 = TR2(ldsT2, 6144, 6656);
  asm volatile("" :: "v"(w84));
  e1_2ap = e1_2ap + e1_2am;
  e1_2bp = e1_2bp + e1_2bm;
  e1_2ap = pm1 ? e1_2ap : A1[4];
  e1_2bp = pm1 ? e1_2bp : A1[5];
  zs1 += e1_2ap;
  SB;
  A0 = MF32(xf[13], w85, A0);
  SB;
  const s16x8 w88 = TR2(ldsT2, 8192, 8704);
  asm volatile("" :: "v"(w85));
  zs1 += e1_2bp;
  hp0[2] = cvtpk(e1_2ap, e1_2bp);
  float e1_3am = A1[6] * LOG2E;
  float e1_3bm = A1[7] * LOG2E;
  e1_3am = __builtin_fminf(e1_3am, 0.0f);
  SB;
  A0 = MF32(xf[14], w86, A0);
  SB;
  const s16x8 w89 = TR2(ldsT2, 10240, 10752);
  asm volatile("" :: "v"(w86));
  e1_3bm = __builtin_fminf(e1_3bm, 0.0f);
  float e1_3ap = A1[6] + 1e-6f;
  float e1_3bp = A1[7] + 1e-6f;
  e1_3am = __builtin_amdgcn_exp2f(e1_3am);
  DMA2(lW, 73728, voffW3, srdW, sW, 663552);
  SB;
  A0 = MF32(xf[15], w87, A0);
  SB;
  const s16x8 w90 = TR2(ldsT2, 12288, 12800);
  asm volatile("" :: "v"(w87));
  e1_3bm = __builtin_amdgcn_exp2f(e1_3bm);
  e1_3ap = __builtin_fmaxf(e1_3ap, 1e-6f);
  e1_3bp = __builtin_fmaxf(e1_3bp, 1e-6f);
  SB;
  A0 = MF32(xf[16], w88, A0);
  SB;
  const s16x8 w91 = TR2(ldsT2, 14336, 14848);
  asm volatile("" :: "v"(w88));
  e1_3ap = e1_3ap + e1_3am;
  e1_3bp = e1_3bp + e1_3bm;
  e1_3ap = pm1 ? e1_3ap : A1[6];
  e1_3bp = pm1 ? e1_3bp : A1[7];
  zs1 += e1_3ap;
  SB;
  A0 = MF32(xf[17], w89, A0);
  SB;
  const s16x8 w92 = TR2(ldsT2, 16384, 16896);
  asm volatile("" :: "v"(w89));
  zs1 += e1_3bp;
  hp0[3] = cvtpk(e1_3ap, e1_3bp);
  float e1_4am = A1[8] * LOG2E;
  SB;
  A0 = MF32(xf[18], w90, A0);
  SB;
  const s16x8 w93 = TR2(ldsT2, 18432, 18944);
  asm volatile("" :: "v"(w90));
  *(LAS u32x4*)(size_t)(tw1 + 0) = hp0;
  float e1_4bm = A1[9] * LOG2E;
  e1_4am = __builtin_fminf(e1_4am, 0.0f);
  e1_4bm = __builtin_fminf(e1_4bm, 0.0f);
  float e1_4ap = A1[8] + 1e-6f;
  float e1_4bp = A1[9] + 1e-6f;
  DMA2(lW, 74752, voffW3, srdW, sW, 691200);
  SB;
  A0 = MF32(xf[19], w91, A0);
  SB;
  const s16x8 w94 = TR2(ldsT2, 20480, 20992);
  asm volatile("" :: "v"(w91));
  e1_4am = __builtin_amdgcn_exp2f(e1_4am);
  e1_4bm = __builtin_amdgcn_exp2f(e1_4bm);
  e1_4ap = __builtin_fmaxf(e1_4ap, 1e-6f);
  SB;
  A0 = MF32(xf[20], w92, A0);
  SB;
  const s16x8 w95 = TR2(ldsT2, 22528, 23040);
  asm volatile("" :: "v"(w92));
  e1_4bp = __builtin_fmaxf(e1_4bp, 1e-6f);
  e1_4ap = e1_4ap + e1_4am;
  e1_4bp = e1_4bp + e1_4bm;
  e1_4ap = pm1 ? e1_4ap : A1[8];
  SB;
  A0 = MF32(xf[21], w93, A0);
  SB;
  const s16x8 w96 = TR2(ldsT0, 24576, 25088);
  asm volatile("" :: "v"(w93));
  e1_4bp = pm1 ? e1_4bp : A1[9];
  zs1 += e1_4ap;
  zs1 += e1_4bp;
  hp1[0] = cvtpk(e1_4ap, e1_4bp);
  float e1_5am = A1[10] * LOG2E;
  SB;
  A0 = MF32(xf[22], w94, A0);
  SB;
  const s16x8 w97 = TR2(ldsT0, 26624, 27136);
  asm volatile("" :: "v"(w94));
  float e1_5bm = A1[11] * LOG2E;
  e1_5am = __builtin_fminf(e1_5am, 0.0f);
  e1_5bm = __builtin_fminf(e1_5bm, 0.0f);
  float e1_5ap = A1[10] + 1e-6f;
  DMA2(lW, 75776, voffW3, srdW, sW, 718848);
  SB;
  A0 = MF32(xf[23], w95, A0);
  SB;
  const s16x8 w98 = TR2(ldsT0, 28672, 29184);
  asm volatile("" :: "v"(w95));
  float e1_5bp = A1[11] + 1e-6f;
  e1_5am = __builtin_amdgcn_exp2f(e1_5am);
  e1_5bm = __builtin_amdgcn_exp2f(e1_5bm);
  SB;
  SB; asm volatile("s_waitcnt vmcnt(3)\n\ts_barrier" :  :: "memory"); SB;
  A0 = MF32(xf[24], w96, A0);
  SB;
  const s16x8 w99 = TR2(ldsT0, 30720, 31232);
  asm volatile("" :: "v"(w96));
  e1_5ap = __builtin_fmaxf(e1_5ap, 1e-6f);
  e1_5bp = __builtin_fmaxf(e1_5bp, 1e-6f);
  e1_5ap = e1_5ap + e1_5am;
  e1_5bp = e1_5bp + e1_5bm;
  e1_5ap = pm1 ? e1_5ap : A1[10];
  SB;
  A0 = MF32(xf[25], w97, A0);
  SB;
  const s16x8 w100 = TR2(ldsT0, 32768, 33280);
  asm volatile("" :: "v"(w97));
  e1_5bp = pm1 ? e1_5bp : A1[11];
  zs1 += e1_5ap;
  zs1 += e1_5bp;
  hp1[1] = cvtpk(e1_5ap, e1_5bp);
  SB;
  A0 = MF32(xf[26], w98, A0);
  SB;
  const s16x8 w101 = TR2(ldsT0, 34816, 35328);
  asm volatile("" :: "v"(w98));
  float e1_6am = A1[12] * LOG2E;
  float e1_6bm = A1[13] * LOG2E;
  e1_6am = __builtin_fminf(e1_6am, 0.0f);
  e1_6bm = __builtin_fminf(e1_6bm, 0.0f);
  float e1_6ap = A1[12] + 1e-6f;
  DMA2(lW, 98304, voffW3, srdW, sW, 1327104);
  SB;
  A0 = MF32(xf[27], w99, A0);
  SB;
  const s16x8 w102 = TR2(ldsT0, 36864, 37376);
  asm volatile("" :: "v"(w99));
  float e1_6bp = A1[13] + 1e-6f;
  e1_6am = __builtin_amdgcn_exp2f(e1_6am);
  e1_6bm = __builtin_amdgcn_exp2f(e1_6bm);
  SB;
  A0 = MF32(xf[28], w100, A0);
  SB;
  const s16x8 w103 = TR2(ldsT0, 38912, 39424);
  asm volatile("" :: "v"(w100));
  e1_6ap = __builtin_fmaxf(e1_6ap, 1e-6f);
  e1_6bp = __builtin_fmaxf(e1_6bp, 1e-6f);
  e1_6ap = e1_6ap + e1_6am;
  e1_6bp = e1_6bp + e1_6bm;
  SB;
  A0 = MF32(xf[29], w101, A0);
  SB;
  const s16x8 w104 = TR2(ldsT0, 40960, 41472);
  asm volatile("" :: "v"(w101));
  e1_6ap = pm1 ? e1_6ap : A1[12];
  e1_6bp = pm1 ? e1_6bp : A1[13];
  zs1 += e1_6ap;
  zs1 += e1_6bp;
  hp1[2] = cvtpk(e1_6ap, e1_6bp);
  SB;
  A0 = MF32(xf[30], w102, A0);
  SB;
  const s16x8 w105 = TR2(ldsT0, 43008, 43520);
  asm volatile("" :: "v"(w102));
  float e1_7am = A1[14] * LOG2E;
  float e1_7bm = A1[15] * LOG2E;
  e1_7am = __builtin_fminf(e1_7am, 0.0f);
  e1_7bm = __builtin_fminf(e1_7bm, 0.0f);
  DMA2(lW, 99328, voffW3, srdW, sW, 1354752);
  SB;
  A0 = MF32(xf[31], w103, A0);
  SB;
  const s16x8 w106 = TR2(ldsT0, 45056, 45568);
  asm volatile("" :: "v"(w103));
  float e1_7ap = A1[14] + 1e-6f;
  float e1_7bp = A1[15] + 1e-6f;
  e1_7am = __builtin_amdgcn_exp2f(e1_7am);
  e1_7bm = __builtin_amdgcn_exp2f(e1_7bm);
  SB;
  A0 = MF32(xf[32], w104, A0);
  SB;
  const s16x8 w107 = TR2(ldsT0, 47104, 47616);
  asm volatile("" :: "v"(w104));
  e1_7ap = __builtin_fmaxf(e1_7ap, 1e-6f);
  e1_7bp = __builtin_fmaxf(e1_7bp, 1e-6f);
  e1_7ap = e1_7ap + e1_7am;
  e1_7bp = e1_7bp + e1_7bm;
  SB;
  A0 = MF32(xf[33], w105, A0);
  SB;
  const s16x8 w108 = TR2(ldsT1, 0, 512);
  asm volatile("" :: "v"(w105));
  e1_7ap = pm1 ? e1_7ap : A1[14];
  e1_7bp = pm1 ? e1_7bp : A1[15];
  zs1 += e1_7ap;
  zs1 += e1_7bp;
  SB;
  A0 = MF32(xf[34], w106, A0);
  SB;
  const s16x8 w109 = TR2(ldsT1, 2048, 2560);
  asm volatile("" :: "v"(w106));
  hp1[3] = cvtpk(e1_7ap, e1_7bp);
  { const auto zp = __builtin_amdgcn_permlane32_swap(__float_as_uint(zs1), __float_as_uint(zs1), false, false); *(LAS float*)(size_t)(16384 + rg * 768 + (4 + cg) * 128 + t31 * 4) = __uint_as_float(zp[0]) + __uint_as_float(zp[1]); }
  DMA2(lW, 100352, voffW3, srdW, sW, 1382400);
  SB;
  A0 = MF32(xf[35], w107, A0);
  SB;
  const s16x8 w110 = TR2(ldsT1, 4096, 4608);
  asm volatile("" :: "v"(w107));
  *(LAS u32x4*)(size_t)(tw1 + 1024) = hp1;
  SB;
  SB; asm volatile("s_waitcnt vmcnt(3)\n\ts_barrier" :  :: "memory"); SB;
  const u32 voffW4 = qW0 + 2528 + (lcA >= 4 ? (u32)(-2592) : 0u);
  A1 = MF32(xf[0], w108, zero16);
  SB;
  const s16x8 w111 = TR2(ldsT1, 6144, 6656);
  asm volatile("" :: "v"(w108));
  SB;
  A1 = MF32(xf[1], w109, A1);
  SB;
  const s16x8 w112 = TR2(ldsT1, 8192, 8704);
  asm volatile("" :: "v"(w109));
  const u32 tw2 = OPQ((cg ? 155648 : 8192) + rg * 2048 + vl16);
  SB;
  A1 = MF32(xf[2], w110, A1);
  SB;
  const s16x8 w113 = TR2(ldsT1, 10240, 10752);
  asm volatile("" :: "v"(w110));
  hp0[0] = cvtb(A0[0], A0[1]);
  DMA2(lW, 24576, voffW4, srdW, sW, 0);
  SB;
  A1 = MF32(xf[3], w111, A1);
  SB;
  const s16x8 w114 = TR2(ldsT1, 12288, 12800);
  asm volatile("" :: "v"(w111));
  SB;
  A1 = MF32(xf[4], w112, A1);
  SB;
  const s16x8 w115 = TR2(ldsT1, 14336, 14848);
  asm volatile("" :: "v"(w112));
  hp0[1] = cvtb(A0[2], A0[3]);
  SB;
  A1 = MF32(xf[5], w113, A1);
  SB;
  const s16x8 w116 = TR2(ldsT1, 16384, 16896);
  asm volatile("" :: "v"(w113));
  SB;
  A1 = MF32(xf[6], w114, A1);
  SB;
  const s16x8 w117 = TR2(ldsT1, 18432, 18944);
  asm volatile("" :: "v"(w114));
  hp0[2] = cvtb(A0[4], A0[5]);
  DMA2(lW, 25600, voffW4, srdW, sW, 27648);
  SB;
  A1 = MF32(xf[7], w115, A1);
  SB;
  const s16x8 w118 = TR2(ldsT1, 20480, 20992);
  asm volatile("" :: "v"(w115));
  SB;
  A1 = MF32(xf[8], w116, A1);
  SB;
  const s16x8 w119 = TR2(ldsT1, 22528, 23040);
  asm volatile("" :: "v"(w116));
  hp0[3] = cvtb(A0[6], A0[7]);
  SB;
  A1 = MF32(xf[9], w117, A1);
  SB;
  const s16x8 w120 = TR2(ldsT1, 24576, 25088);
  asm volatile("" :: "v"(w117));
  SB;
  A1 = MF32(xf[10], w118, A1);
  SB;
  const s16x8 w121 = TR2(ldsT1, 26624, 27136);
  asm volatile("" :: "v"(w118));
  DMA2(lW, 26624, voffW4, srdW, sW, 55296);
  SB;
  A1 = MF32(xf[11], w119, A1);
  SB;
  const s16x8 w122 = TR2(ldsT1, 28672, 29184);
  asm volatile("" :: "v"(w119));
  *(LAS u32x4*)(size_t)(tw2 + 0) = hp0;
  hp1[0] = cvtb(A0[8], A0[9]);
  SB;
  SB; asm volatile("s_waitcnt vmcnt(3)\n\ts_barrier" :  :: "memory"); SB;
  const u32 sKV = 2097152 + (j * 16 + r) * 18944;
  A1 = MF32(xf[12], w120, A1);
  SB;
  const s16x8 w123 = TR2(ldsT1, 30720, 31232);
  asm volatile("" :: "v"(w120));
  SB;
  A1 = MF32(xf[13], w121, A1);
  SB;
  const s16x8 w124 = TR2(ldsT1, 32768, 33280);
  asm volatile("" :: "v"(w121));
  hp1[1] = cvtb(A0[10], A0[11]);
  SB;
  A1 = MF32(xf[14], w122, A1);
  SB;
  const s16x8 w125 = TR2(ldsT1, 34816, 35328);
  asm volatile("" :: "v"(w122));
  DMA2(lW, 49152, voffW4, srdW, sW, 663552);
  SB;
  A1 = MF32(xf[15], w123, A1);
  SB;
  const s16x8 w126 = TR2(ldsT1, 36864, 37376);
  asm volatile("" :: "v"(w123));
  hp1[2] = cvtb(A0[12], A0[13]);
  SB;
  A1 = MF32(xf[16], w124, A1);
  SB;
  const s16x8 w127 = TR2(ldsT1, 38912, 39424);
  asm volatile("" :: "v"(w124));
  SB;
  A1 = MF32(xf[17], w125, A1);
  SB;
  const s16x8 w128 = TR2(ldsT1, 40960, 41472);
  asm volatile("" :: "v"(w125));
  hp1[3] = cvtb(A0[14], A0[15]);
  SB;
  A1 = MF32(xf[18], w126, A1);
  SB;
  const s16x8 w129 = TR2(ldsT1, 43008, 43520);
  asm volatile("" :: "v"(w126));
  DMA2(lW, 50176, voffW4, srdW, sW, 691200);
  SB;
  A1 = MF32(xf[19], w127, A1);
  SB;
  const s16x8 w130 = TR2(ldsT1, 45056, 45568);
  asm volatile("" :: "v"(w127));
  SB;
  A1 = MF32(xf[20], w128, A1);
  SB;
  const s16x8 w131 = TR2(ldsT1, 47104, 47616);
  asm volatile("" :: "v"(w128));
  *(LAS u32x4*)(size_t)(tw2 + 1024) = hp1;
  SB;
  A1 = MF32(xf[21], w129, A1);
  SB;
  const s16x8 w132 = TR2(ldsT2, 0, 512);
  asm volatile("" :: "v"(w129));
  SB;
  A1 = MF32(xf[22], w130, A1);
  SB;
  const s16x8 w133 = TR2(ldsT2, 2048, 2560);
  asm volatile("" :: "v"(w130));
  DMA2(lW, 51200, voffW4, srdW, sW, 718848);
  SB;
  A1 = MF32(xf[23], w131, A1);
  SB;
  const s16x8 w134 = TR2(ldsT2, 4096, 4608);
  asm volatile("" :: "v"(w131));
  SB;
  SB; asm volatile("s_waitcnt vmcnt(3)\n\ts_barrier" :  :: "memory"); SB;
  A1 = MF32(xf[24], w132, A1);
  SB;
  const s16x8 w135 = TR2(ldsT2, 6144, 6656);
  asm volatile("" :: "v"(w132));
  const u32 r1a = OPQ((rg == 0 ? 131072 : rg == 1 ? 131072 : rg == 2 ? 139264 : 139264) + vl16), r1b = OPQ((rg == 0 ? 0 : rg == 1 ? 147456 : rg == 2 ? 147456 : 8192) + vl16), r1t = rg, r1v = cg ? OOB : vl16;
  const s16x8 r1a0 = *(LAS s16x8*)(size_t)(r1a + 0), r1b0 = *(LAS s16x8*)(size_t)(r1b + 0);

  SB;
  A1 = MF32(xf[25], w133, A1);
  SB;
  const s16x8 w136 = TR2(ldsT2, 8192, 8704);
  asm volatile("" :: "v"(w133));
  const s16x8 r1a1 = *(LAS s16x8*)(size_t)(r1a + 1024), r1b1 = *(LAS s16x8*)(size_t)(r1b + 1024);
  f32x16 r1d = MF32(r1a0, r1b0, zero16);
  SB;
  A1 = MF32(xf[26], w134, A1);
  SB;
  const s16x8 w137 = TR2(ldsT2, 10240, 10752);
  asm volatile("" :: "v"(w134));
  const s16x8 r1a2 = *(LAS s16x8*)(size_t)(r1a + 2048), r1b2 = *(LAS s16x8*)(size_t)(r1b + 2048);
  r1d = MF32(r1a1, r1b1, r1d);
  DMA2(lW, 73728, voffW4, srdW, sW, 1327104);
  SB;
  A1 = MF32(xf[27], w135, A1);
  SB;
  const s16x8 w138 = TR2(ldsT2, 12288, 12800);
  asm volatile("" :: "v"(w135));
  const s16x8 r1a3 = *(LAS s16x8*)(size_t)(r1a + 3072), r1b3 = *(LAS s16x8*)(size_t)(r1b + 3072);
  r1d = MF32(r1a2, r1b2, r1d);
  SB;
  A1 = MF32(xf[28], w136, A1);
  SB;
  const s16x8 w139 = TR2(ldsT2, 14336, 14848);
  asm volatile("" :: "v"(w136));
  const s16x8 r1a4 = *(LAS s16x8*)(size_t)(r1a + 4096), r1b4 = *(LAS s16x8*)(size_t)(r1b + 4096);
  r1d = MF32(r1a3, r1b3, r1d);
  SB;
  A1 = MF32(xf[29], w137, A1);
  SB;
  const s16x8 w140 = TR2(ldsT2, 16384, 16896);
  asm volatile("" :: "v"(w137));
  const s16x8 r1a5 = *(LAS s16x8*)(size_t)(r1a + 5120), r1b5 = *(LAS s16x8*)(size_t)(r1b + 5120);
  r1d = MF32(r1a4, r1b4, r1d);
  const s16x8 r1a6 = *(LAS s16x8*)(size_t)(r1a + 6144), r1b6 = *(LAS s16x8*)(size_t)(r1b + 6144);
  SB;
  A1 = MF32(xf[30], w138, A1);
  SB;
  const s16x8 w141 = TR2(ldsT2, 18432, 18944);
  asm volatile("" :: "v"(w138));
  r1d = MF32(r1a5, r1b5, r1d);
  const s16x8 r1a7 = *(LAS s16x8*)(size_t)(r1a + 7168), r1b7 = *(LAS s16x8*)(size_t)(r1b + 7168);
  DMA2(lW, 74752, voffW4, srdW, sW, 1354752);
  SB;
  A1 = MF32(xf[31], w139, A1);
  SB;
  const s16x8 w142 = TR2(ldsT2, 20480, 20992);
  asm volatile("" :: "v"(w139));
  r1d = MF32(r1a6, r1b6, r1d);
  SB;
  A1 = MF32(xf[32], w140, A1);
  SB;
  const s16x8 w143 = TR2(ldsT2, 22528, 23040);
  asm volatile("" :: "v"(w140));
  r1d = MF32(r1a7, r1b7, r1d);

  SB;
  A1 = MF32(xf[33], w141, A1);
  SB;
  const s16x8 w144 = TR2(ldsT0, 24576, 25088);
  asm volatile("" :: "v"(w141));
  u32x4 r1k0; r1k0[0] = cvtb(r1d[0], r1d[1]); r1k0[1] = cvtb(r1d[2], r1d[3]); r1k0[2] = cvtb(r1d[4], r1d[5]); r1k0[3] = cvtb(r1d[6], r1d[7]);
  SB;
  A1 = MF32(xf[34], w142, A1);
  SB;
  const s16x8 w145 = TR2(ldsT0, 26624, 27136);
  asm volatile("" :: "v"(w142));
  u32x4 r1k1; r1k1[0] = cvtb(r1d[8], r1d[9]); r1k1[1] = cvtb(r1d[10], r1d[11]); r1k1[2] = cvtb(r1d[12], r1d[13]); r1k1[3] = cvtb(r1d[14], r1d[15]);
  DMA2(lW, 75776, voffW4, srdW, sW, 1382400);
  SB;
  A1 = MF32(xf[35], w143, A1);
  SB;
  const s16x8 w146 = TR2(ldsT0, 28672, 29184);
  asm volatile("" :: "v"(w143));
  STORE4(r1k0, r1v, srdWS, sKV + r1t * 2048, 0);
  STORE4(r1k1, r1v, srdWS, sKV + r1t * 2048, 1024);
  SB;
  SB; asm volatile("s_waitcnt vmcnt(5)\n\ts_barrier" :  :: "memory"); SB;
  const u32 voffW5 = qW0 + 64;
  A0 = MF32(xf[0], w144, zero16);
  SB;
  const s16x8 w147 = TR2(ldsT0, 30720, 31232);
  asm volatile("" :: "v"(w144));
  SB;
  A0 = MF32(xf[1], w145, A0);
  SB;
  const s16x8 w148 = TR2(ldsT0, 32768, 33280);
  asm volatile("" :: "v"(w145));
  const u32 tw3 = OPQ((cg ? 139264 : 131072) + rg * 2048 + vl16);
  float e3_0am = A1[0] * LOG2E;
  float e3_0bm = A1[1] * LOG2E;
  e3_0am = __builtin_fminf(e3_0am, 0.0f);
  e3_0bm = __builtin_fminf(e3_0bm, 0.0f);
  SB;
  A0 = MF32(xf[2], w146, A0);
  SB;
  const s16x8 w149 = TR2(ldsT0, 34816, 35328);
  asm volatile("" :: "v"(w146));
  float e3_0ap = A1[0] + 1e-6f;
  float e3_0bp = A1[1] + 1e-6f;
  e3_0am = __builtin_amdgcn_exp2f(e3_0am);
  DMA2(lW, 98304, voffW5, srdW, sW, 0);
  SB;
  A0 = MF32(xf[3], w147, A0);
  SB;
  const s16x8 w150 = TR2(ldsT0, 36864, 37376);
  asm volatile("" :: "v"(w147));
  e3_0bm = __builtin_amdgcn_exp2f(e3_0bm);
  e3_0ap = __builtin_fmaxf(e3_0ap, 1e-6f);
  e3_0bp = __builtin_fmaxf(e3_0bp, 1e-6f);
  SB;
  A0 = MF32(xf[4], w148, A0);
  SB;
  const s16x8 w151 = TR2(ldsT0, 38912, 39424);
  asm volatile("" :: "v"(w148));
  e3_0ap = e3_0ap + e3_0am;
  e3_0bp = e3_0bp + e3_0bm;
  float zs3 = e3_0ap + e3_0bp;
  hp0[0] = cvtpk(e3_0ap, e3_0bp);
  SB;
  A0 = MF32(xf[5], w149, A0);
  SB;
  const s16x8 w152 = TR2(ldsT0, 40960, 41472);
  asm volatile("" :: "v"(w149));
  float e3_1am = A1[2] * LOG2E;
  float e3_1bm = A1[3] * LOG2E;
  e3_1am = __builtin_fminf(e3_1am, 0.0f);
  e3_1bm = __builtin_fminf(e3_1bm, 0.0f);
  SB;
  A0 = MF32(xf[6], w150, A0);
  SB;
  const s16x8 w153 = TR2(ldsT0, 43008, 43520);
  asm volatile("" :: "v"(w150));
  float e3_1ap = A1[2] + 1e-6f;
  float e3_1bp = A1[3] + 1e-6f;
  e3_1am = __builtin_amdgcn_exp2f(e3_1am);
  DMA2(lW, 99328, voffW5, srdW, sW, 27648);
  SB;
  A0 = MF32(xf[7], w151, A0);
  SB;
  const s16x8 w154 = TR2(ldsT0, 45056, 45568);
  asm volatile("" :: "v"(w151));
  e3_1bm = __builtin_amdgcn_exp2f(e3_1bm);
  e3_1ap = __builtin_fmaxf(e3_1ap, 1e-6f);
  e3_1bp = __builtin_fmaxf(e3_1bp, 1e-6f);
  SB;
  A0 = MF32(xf[8], w152, A0);
  SB;
  const s16x8 w155 = TR2(ldsT0, 47104, 47616);
  asm volatile("" :: "v"(w152));
  e3_1ap = e3_1ap + e3_1am;
  e3_1bp = e3_1bp + e3_1bm;
  zs3 += e3_1ap;
  zs3 += e3_1bp;
  hp0[1] = cvtpk(e3_1ap, e3_1bp);
  SB;
  A0 = MF32(xf[9], w153, A0);
  SB;
  const s16x8 w156 = TR2(ldsT1, 0, 512);
  asm volatile("" :: "v"(w153));
  float e3_2am = A1[4] * LOG2E;
  float e3_2bm = A1[5] * LOG2E;
  e3_2am = __builtin_fminf(e3_2am, 0.0f);
  e3_2bm = __builtin_fminf(e3_2bm, 0.0f);
  SB;
  A0 = MF32(xf[10], w154, A0);
  SB;
  const s16x8 w157 = TR2(ldsT1, 2048, 2560);
  asm volatile("" :: "v"(w154));
  float e3_2ap = A1[4] + 1e-6f;
  float e3_2bp = A1[5] + 1e-6f;
  e3_2am = __builtin_amdgcn_exp2f(e3_2am);
  DMA2(lW, 100352, voffW5, srdW, sW, 55296);
  SB;
  A0 = MF32(xf[11], w155, A0);
  SB;
  const s16x8 w158 = TR2(ldsT1, 4096, 4608);
  asm volatile("" :: "v"(w155));
  e3_2bm = __builtin_amdgcn_exp2f(e3_2bm);
  e3_2ap = __builtin_fmaxf(e3_2ap, 1e-6f);
  e3_2bp = __builtin_fmaxf(e3_2bp, 1e-6f);
  SB;
  SB; asm volatile("s_waitcnt vmcnt(5)\n\ts_barrier" :  :: "memory"); SB;
  A0 = MF32(xf[12], w156, A0);
  SB;
  const s16x8 w159 = TR2(ldsT1, 6144, 6656);
  asm volatile("" :: "v"(w156));
  e3_2ap = e3_2ap + e3_2am;
  e3_2bp = e3_2bp + e3_2bm;
  zs3 += e3_2ap;
  zs3 += e3_2bp;
  SB;
  A0 = MF32(xf[13], w157, A0);
  SB;
  const s16x8 w160 = TR2(ldsT1, 8192, 8704);
  asm volatile("" :: "v"(w157));
  hp0[2] = cvtpk(e3_2ap, e3_2bp);
  float e3_3am = A1[6] * LOG2E;
  float e3_3bm = A1[7] * LOG2E;
  e3_3am = __builtin_fminf(e3_3am, 0.0f);
  SB;
  A0 = MF32(xf[14], w158, A0);
  SB;
  const s16x8 w161 = TR2(ldsT1, 10240, 10752);
  asm volatile("" :: "v"(w158));
  e3_3bm = __builtin_fminf(e3_3bm, 0.0f);
  float e3_3ap = A1[6] + 1e-6f;
  float e3_3bp = A1[7] + 1e-6f;
  e3_3am = __builtin_amdgcn_exp2f(e3_3am);
  DMA2(lW, 24576, voffW5, srdW, sW, 663552);
  SB;
  A0 = MF32(xf[15], w159, A0);
  SB;
  const s16x8 w162 = TR2(ldsT1, 12288, 12800);
  asm volatile("" :: "v"(w159));
  e3_3bm = __builtin_amdgcn_exp2f(e3_3bm);
  e3_3ap = __builtin_fmaxf(e3_3ap, 1e-6f);
  SB;
  A0 = MF32(xf[16], w160, A0);
  SB;
  const s16x8 w163 = TR2(ldsT1, 14336, 14848);
  asm volatile("" :: "v"(w160));
  e3_3bp = __builtin_fmaxf(e3_3bp, 1e-6f);
  e3_3ap = e3_3ap + e3_3am;
  e3_3bp = e3_3bp + e3_3bm;
  zs3 += e3_3ap;
  zs3 += e3_3bp;
  SB;
  A0 = MF32(xf[17], w161, A0);
  SB;
  const s16x8 w164 = TR2(ldsT1, 16384, 16896);
  asm volatile("" :: "v"(w161));
  hp0[3] = cvtpk(e3_3ap, e3_3bp);
  float e3_4am = A1[8] * LOG2E;
  float e3_4bm = A1[9] * LOG2E;
  SB;
  A0 = MF32(xf[18], w162, A0);
  SB;
  const s16x8 w165 = TR2(ldsT1, 18432, 18944);
  asm volatile("" :: "v"(w162));
  *(LAS u32x4*)(size_t)(tw3 + 0) = hp0;
  e3_4am = __builtin_fminf(e3_4am, 0.0f);
  e3_4bm = __builtin_fminf(e3_4bm, 0.0f);
  float e3_4ap = A1[8] + 1e-6f;
  float e3_4bp = A1[9] + 1e-6f;
  DMA2(lW, 25600, voffW5, srdW, sW, 691200);
  SB;
  A0 = MF32(xf[19], w163, A0);
  SB;
  const s16x8 w166 = TR2(ldsT1, 20480, 20992);
  asm volatile("" :: "v"(w163));
  e3_4am = __builtin_amdgcn_exp2f(e3_4am);
  e3_4bm = __builtin_amdgcn_exp2f(e3_4bm);
  SB;
  A0 = MF32(xf[20], w164, A0);
  SB;
  const s16x8 w167 = TR2(ldsT1, 22528, 23040);
  asm volatile("" :: "v"(w164));
  e3_4ap = __builtin_fmaxf(e3_4ap, 1e-6f);
  e3_4bp = __builtin_fmaxf(e3_4bp, 1e-6f);
  e3_4ap = e3_4ap + e3_4am;
  e3_4bp = e3_4bp + e3_4bm;
  SB;
  A0 = MF32(xf[21], w165, A0);
  SB;
  const s16x8 w168 = TR2(ldsT1, 24576, 25088);
  asm volatile("" :: "v"(w165));
  zs3 += e3_4ap;
  zs3 += e3_4bp;
  hp1[0] = cvtpk(e3_4ap, e3_4bp);
  float e3_5am = A1[10] * LOG2E;
  SB;
  A0 = MF32(xf[22], w166, A0);
  SB;
  const s16x8 w169 = TR2(ldsT1, 26624, 27136);
  asm volatile("" :: "v"(w166));
  float e3_5bm = A1[11] * LOG2E;
  e3_5am = __builtin_fminf(e3_5am, 0.0f);
  e3_5bm = __builtin_fminf(e3_5bm, 0.0f);
  float e3_5ap = A1[10] + 1e-6f;
  DMA2(lW, 26624, voffW5, srdW, sW, 718848);
  SB;
  A0 = MF32(xf[23], w167, A0);
  SB;
  const s16x8 w170 = TR2(ldsT1, 28672, 29184);
  asm volatile("" :: "v"(w167));
  float e3_5bp = A1[11] + 1e-6f;
  e3_5am = __builtin_amdgcn_exp2f(e3_5am);
  e3_5bm = __builtin_amdgcn_exp2f(e3_5bm);
  SB;
  SB; asm volatile("s_waitcnt vmcnt(3)\n\ts_barrier" :  :: "memory"); SB;
  A0 = MF32(xf[24], w168, A0);
  SB;
  const s16x8 w171 = TR2(ldsT1, 30720, 31232);
  asm volatile("" :: "v"(w168));
  e3_5ap = __builtin_fmaxf(e3_5ap, 1e-6f);
  e3_5bp = __builtin_fmaxf(e3_5bp, 1e-6f);
  e3_5ap = e3_5ap + e3_5am;
  e3_5bp = e3_5bp + e3_5bm;
  SB;
  A0 = MF32(xf[25], w169, A0);
  SB;
  const s16x8 w172 = TR2(ldsT1, 32768, 33280);
  asm volatile("" :: "v"(w169));
  zs3 += e3_5ap;
  zs3 += e3_5bp;
  hp1[1] = cvtpk(e3_5ap, e3_5bp);
  float e3_6am = A1[12] * LOG2E;
  SB;
  A0 = MF32(xf[26], w170, A0);
  SB;
  const s16x8 w173 = TR2(ldsT1, 34816, 35328);
  asm volatile("" :: "v"(w170));
  float e3_6bm = A1[13] * LOG2E;
  e3_6am = __builtin_fminf(e3_6am, 0.0f);
  e3_6bm = __builtin_fminf(e3_6bm, 0.0f);
  float e3_6ap = A1[12] + 1e-6f;
  DMA2(lW, 49152, voffW5, srdW, sW, 1327104);
  SB;
  A0 = MF32(xf[27], w171, A0);
  SB;
  const s16x8 w174 = TR2(ldsT1, 36864, 37376);
  asm volatile("" :: "v"(w171));
  float e3_6bp = A1[13] + 1e-6f;
  e3_6am = __builtin_amdgcn_exp2f(e3_6am);
  e3_6bm = __builtin_amdgcn_exp2f(e3_6bm);
  SB;
  A0 = MF32(xf[28], w172, A0);
  SB;
  const s16x8 w175 = TR2(ldsT1, 38912, 39424);
  asm volatile("" :: "v"(w172));
  e3_6ap = __builtin_fmaxf(e3_6ap, 1e-6f);
  e3_6bp = __builtin_fmaxf(e3_6bp, 1e-6f);
  e3_6ap = e3_6ap + e3_6am;
  SB;
  A0 = MF32(xf[29], w173, A0);
  SB;
  const s16x8 w176 = TR2(ldsT1, 40960, 41472);
  asm volatile("" :: "v"(w173));
  e3_6bp = e3_6bp + e3_6bm;
  zs3 += e3_6ap;
  zs3 += e3_6bp;
  hp1[2] = cvtpk(e3_6ap, e3_6bp);
  SB;
  A0 = MF32(xf[30], w174, A0);
  SB;
  const s16x8 w177 = TR2(ldsT1, 43008, 43520);
  asm volatile("" :: "v"(w174));
  float e3_7am = A1[14] * LOG2E;
  float e3_7bm = A1[15] * LOG2E;
  e3_7am = __builtin_fminf(e3_7am, 0.0f);
  e3_7bm = __builtin_fminf(e3_7bm, 0.0f);
  DMA2(lW, 50176, voffW5, srdW, sW, 1354752);
  SB;
  A0 = MF32(xf[31], w175, A0);
  SB;
  const s16x8 w178 = TR2(ldsT1, 45056, 45568);
  asm volatile("" :: "v"(w175));
  float e3_7ap = A1[14] + 1e-6f;
  float e3_7bp = A1[15] + 1e-6f;
  e3_7am = __builtin_amdgcn_exp2f(e3_7am);
  SB;
  A0 = MF32(xf[32], w176, A0);
  SB;
  const s16x8 w179 = TR2(ldsT1, 47104, 47616);
  asm volatile("" :: "v"(w176));
  e3_7bm = __builtin_amdgcn_exp2f(e3_7bm);
  e3_7ap = __builtin_fmaxf(e3_7ap, 1e-6f);
  e3_7bp = __builtin_fmaxf(e3_7bp, 1e-6f);
  e3_7ap = e3_7ap + e3_7am;
  SB;
  A0 = MF32(xf[33], w177, A0);
  SB;
  const s16x8 w180 = TR2(ldsT2, 0, 512);
  asm volatile("" :: "v"(w177));
  e3_7bp = e3_7bp + e3_7bm;
  zs3 += e3_7ap;
  zs3 += e3_7bp;
  hp1[3] = cvtpk(e3_7ap, e3_7bp);
  SB;
  A0 = MF32(xf[34], w178, A0);
  SB;
  const s16x8 w181 = TR2(ldsT2, 2048, 2560);
  asm volatile("" :: "v"(w178));
  { const auto zp = __builtin_amdgcn_permlane32_swap(__float_as_uint(zs3), __float_as_uint(zs3), false, false); *(LAS float*)(size_t)(16384 + rg * 768 + (2 + cg) * 128 + t31 * 4) = __uint_as_float(zp[0]) + __uint_as_float(zp[1]); }
  DMA2(lW, 51200, voffW5, srdW, sW, 1382400);
  SB;
  A0 = MF32(xf[35], w179, A0);
  SB;
  const s16x8 w182 = TR2(ldsT2, 4096, 4608);
  asm volatile("" :: "v"(w179));
  *(LAS u32x4*)(size_t)(tw3 + 1024) = hp1;
  SB;
  SB; asm volatile("s_waitcnt vmcnt(3)\n\ts_barrier" :  :: "memory"); SB;
  const u32 voffW6 = qW0 + 192 + (lcA >= 6 ? (u32)(-32) : 0u);
  A1 = MF32(xf[0], w180, zero16);
  SB;
  const s16x8 w183 = TR2(ldsT2, 6144, 6656);
  asm volatile("" :: "v"(w180));
  SB;
  A1 = MF32(xf[1], w181, A1);
  SB;
  const s16x8 w184 = TR2(ldsT2, 8192, 8704);
  asm volatile("" :: "v"(w181));
  const u32 tw4 = OPQ((cg ? 122880 : 147456) + rg * 2048 + vl16);
  const u32 tq4 = cg ? 122880 + rg * 2048 : 20480;
  hp0[0] = cvtb(A0[0], A0[1]);
  hp0[1] = cvtb(A0[2], A0[3]);
  hp0[2] = cvtb(A0[4], A0[5]);
  hp0[3] = cvtb(A0[6], A0[7]);
   *(LAS u32x4*)(size_t)(tw4 + 0) = hp0;
  SB;
  A1 = MF32(xf[2], w182, A1);
  SB;
  const s16x8 w185 = TR2(ldsT2, 10240, 10752);
  asm volatile("" :: "v"(w182));
  hp1[0] = cvtb(A0[8], A0[9]);
  hp1[1] = cvtb(A0[10], A0[11]);
  hp1[2] = cvtb(A0[12], A0[13]);
  hp1[3] = cvtb(A0[14], A0[15]);
   *(LAS u32x4*)(size_t)(tw4 + 1024) = hp1;
  float e4_0am = A0[0] * LOG2E;
  DMA2(lW, 73728, voffW6, srdW, sW, 0);
  SB;
  A1 = MF32(xf[3], w183, A1);
  SB;
  const s16x8 w186 = TR2(ldsT2, 12288, 12800);
  asm volatile("" :: "v"(w183));
  float e4_0bm = A0[1] * LOG2E;
  e4_0am = __builtin_fminf(e4_0am, 0.0f);
  e4_0bm = __builtin_fminf(e4_0bm, 0.0f);
  float e4_0ap = A0[0] + 1e-6f;
  float e4_0bp = A0[1] + 1e-6f;
  SB;
  A1 = MF32(xf[4], w184, A1);
  SB;
  const s16x8 w187 = TR2(ldsT2, 14336, 14848);
  asm volatile("" :: "v"(w184));
  e4_0am = __builtin_amdgcn_exp2f(e4_0am);
  e4_0bm = __builtin_amdgcn_exp2f(e4_0bm);
  e4_0ap = __builtin_fmaxf(e4_0ap, 1e-6f);
  e4_0bp = __builtin_fmaxf(e4_0bp, 1e-6f);
  SB;
  A1 = MF32(xf[5], w185, A1);
  SB;
  const s16x8 w188 = TR2(ldsT2, 16384, 16896);
  asm volatile("" :: "v"(w185));
  e4_0ap = e4_0ap + e4_0am;
  e4_0bp = e4_0bp + e4_0bm;
  hp0[0] = cvtpk(e4_0ap, e4_0bp);
  float e4_1am = A0[2] * LOG2E;
  float e4_1bm = A0[3] * LOG2E;
  e4_1am = __builtin_fminf(e4_1am, 0.0f);
  SB;
  A1 = MF32(xf[6], w186, A1);
  SB;
  const s16x8 w189 = TR2(ldsT2, 18432, 18944);
  asm volatile("" :: "v"(w186));
  e4_1bm = __builtin_fminf(e4_1bm, 0.0f);
  float e4_1ap = A0[2] + 1e-6f;
  float e4_1bp = A0[3] + 1e-6f;
  e4_1am = __builtin_amdgcn_exp2f(e4_1am);
  DMA2(lW, 74752, voffW6, srdW, sW, 27648);
  SB;
  A1 = MF32(xf[7], w187, A1);
  SB;
  const s16x8 w190 = TR2(ldsT2, 20480, 20992);
  asm volatile("" :: "v"(w187));
  e4_1bm = __builtin_amdgcn_exp2f(e4_1bm);
  e4_1ap = __builtin_fmaxf(e4_1ap, 1e-6f);
  e4_1bp = __builtin_fmaxf(e4_1bp, 1e-6f);
  e4_1ap = e4_1ap + e4_1am;
  e4_1bp = e4_1bp + e4_1bm;
  hp0[1] = cvtpk(e4_1ap, e4_1bp);
  SB;
  A1 = MF32(xf[8], w188, A1);
  SB;
  const s16x8 w191 = TR2(ldsT2, 22528, 23040);
  asm volatile("" :: "v"(w188));
  { u32x2 hq; hq[0] = hp0[0]; hq[1] = hp0[1]; *(LAS u32x2*)(size_t)(tq4 + (qa ^ 0)) = hq; }
  float e4_2am = A0[4] * LOG2E;
  float e4_2bm = A0[5] * LOG2E;
  e4_2am = __builtin_fminf(e4_2am, 0.0f);
  SB;
  A1 = MF32(xf[9], w189, A1);
  SB;
  const s16x8 w192 = TR2(ldsT0, 24576, 25088);
  asm volatile("" :: "v"(w189));
  e4_2bm = __builtin_fminf(e4_2bm, 0.0f);
  float e4_2ap = A0[4] + 1e-6f;
  float e4_2bp = A0[5] + 1e-6f;
  e4_2am = __builtin_amdgcn_exp2f(e4_2am);
  e4_2bm = __builtin_amdgcn_exp2f(e4_2bm);
  SB;
  A1 = MF32(xf[10], w190, A1);
  SB;
  const s16x8 w193 = TR2(ldsT0, 26624, 27136);
  asm volatile("" :: "v"(w190));
  e4_2ap = __builtin_fmaxf(e4_2ap, 1e-6f);
  e4_2bp = __builtin_fmaxf(e4_2bp, 1e-6f);
  e4_2ap = e4_2ap + e4_2am;
  e4_2bp = e4_2bp + e4_2bm;
  hp0[2] = cvtpk(e4_2ap, e4_2bp);
  DMA2(lW, 75776, voffW6, srdW, sW, 55296);
  SB;
  A1 = MF32(xf[11], w191, A1);
  SB;
  const s16x8 w194 = TR2(ldsT0, 28672, 29184);
  asm volatile("" :: "v"(w191));
  SB;
  SB; asm volatile("s_waitcnt vmcnt(3)\n\ts_barrier" :  :: "memory"); SB;
  A1 = MF32(xf[12], w192, A1);
  SB;
  const s16x8 w195 = TR2(ldsT0, 30720, 31232);
  asm volatile("" :: "v"(w192));
  const u32 r2a = OPQ((cg ? (rg == 0 ? 131072 : rg == 1 ? 139264 : rg == 2 ? 139264 : 0) : 131072) + vl16), r2b = OPQ((cg ? (rg == 0 ? 155648 : rg == 1 ? 155648 : rg == 2 ? 147456 : 147456) : 8192) + vl16), r2t = cg ? 5 + rg : 4, r2v = (cg || rg == 0) ? vl16 : OOB;
  const s16x8 r2a0 = *(LAS s16x8*)(size_t)(r2a + 0), r2b0 = *(LAS s16x8*)(size_t)(r2b + 0);

  SB;
  A1 = MF32(xf[13], w193, A1);
  SB;
  const s16x8 w196 = TR2(ldsT0, 32768, 33280);
  asm volatile("" :: "v"(w193));
  const s16x8 r2a1 = *(LAS s16x8*)(size_t)(r2a + 1024), r2b1 = *(LAS s16x8*)(size_t)(r2b + 1024);
  const u32x4 zv = z_reduce(16384, ln);
  SB;
  A1 = MF32(xf[14], w194, A1);
  SB;
  const s16x8 w197 = TR2(ldsT0, 34816, 35328);
  asm volatile("" :: "v"(w194));
  f32x16 r2d = MF32(r2a0, r2b0, zero16);
  const s16x8 r2a2 = *(LAS s16x8*)(size_t)(r2a + 2048), r2b2 = *(LAS s16x8*)(size_t)(r2b + 2048);
  DMA2(lW, 98304, voffW6, srdW, sW, 663552);
  SB;
  A1 = MF32(xf[15], w195, A1);
  SB;
  const s16x8 w198 = TR2(ldsT0, 36864, 37376);
  asm volatile("" :: "v"(w195));
  r2d = MF32(r2a1, r2b1, r2d);
  const s16x8 r2a3 = *(LAS s16x8*)(size_t)(r2a + 3072), r2b3 = *(LAS s16x8*)(size_t)(r2b + 3072);
  SB;
  A1 = MF32(xf[16], w196, A1);
  SB;
  const s16x8 w199 = TR2(ldsT0, 38912, 39424);
  asm volatile("" :: "v"(w196));
  STORE4(zv, (w == 1 && ln < 20) ? vl16 : OOB, srdWS, sKV + 18432, 0);
  r2d = MF32(r2a2, r2b2, r2d);
  const s16x8 r2a4 = *(LAS s16x8*)(size_t)(r2a + 4096), r2b4 = *(LAS s16x8*)(size_t)(r2b + 4096);
  SB;
  A1 = MF32(xf[17], w197, A1);
  SB;
  const s16x8 w200 = TR2(ldsT0, 40960, 41472);
  asm volatile("" :: "v"(w197));
  r2d = MF32(r2a3, r2b3, r2d);
  const s16x8 r2a5 = *(LAS s16x8*)(size_t)(r2a + 5120), r2b5 = *(LAS s16x8*)(size_t)(r2b + 5120);
  SB;
  A1 = MF32(xf[18], w198, A1);
  SB;
  const s16x8 w201 = TR2(ldsT0, 43008, 43520);
  asm volatile("" :: "v"(w198));
  r2d = MF32(r2a4, r2b4, r2d);
  const s16x8 r2a6 = *(LAS s16x8*)(size_t)(r2a + 6144), r2b6 = *(LAS s16x8*)(size_t)(r2b + 6144);
  r2d = MF32(r2a5, r2b5, r2d);
  DMA2(lW, 99328, voffW6, srdW, sW, 691200);
  SB;
  A1 = MF32(xf[19], w199, A1);
  SB;
  const s16x8 w202 = TR2(ldsT0, 45056, 45568);
  asm volatile("" :: "v"(w199));
  const s16x8 r2a7 = *(LAS s16x8*)(size_t)(r2a + 7168), r2b7 = *(LAS s16x8*)(size_t)(r2b + 7168);
  r2d = MF32(r2a6, r2b6, r2d);
  SB;
  A1 = MF32(xf[20], w200, A1);
  SB;
  const s16x8 w203 = TR2(ldsT0, 47104, 47616);
  asm volatile("" :: "v"(w200));
  r2d = MF32(r2a7, r2b7, r2d);
  SB;
  A1 = MF32(xf[21], w201, A1);
  SB;
  const s16x8 w204 = TR2(ldsT1, 0, 512);
  asm volatile("" :: "v"(w201));

  u32x4 r2k0; r2k0[0] = cvtb(r2d[0], r2d[1]); r2k0[1] = cvtb(r2d[2], r2d[3]); r2k0[2] = cvtb(r2d[4], r2d[5]); r2k0[3] = cvtb(r2d[6], r2d[7]);
  SB;
  A1 = MF32(xf[22], w202, A1);
  SB;
  const s16x8 w205 = TR2(ldsT1, 2048, 2560);
  asm volatile("" :: "v"(w202));
  u32x4 r2k1; r2k1[0] = cvtb(r2d[8], r2d[9]); r2k1[1] = cvtb(r2d[10], r2d[11]); r2k1[2] = cvtb(r2d[12], r2d[13]); r2k1[3] = cvtb(r2d[14], r2d[15]);
  DMA2(lW, 100352, voffW6, srdW, sW, 718848);
  SB;
  A1 = MF32(xf[23], w203, A1);
  SB;
  const s16x8 w206 = TR2(ldsT1, 4096, 4608);
  asm volatile("" :: "v"(w203));
  STORE4(r2k0, r2v, srdWS, sKV + r2t * 2048, 0);
  STORE4(r2k1, r2v, srdWS, sKV + r2t * 2048, 1024);
  SB;
  SB; asm volatile("s_waitcnt vmcnt(6)\n\ts_barrier" :  :: "memory"); SB;
  const u32 fo = 2 * w + (ln & 1), vpo = (j * 16 + (fo == r ? fo ^ 1 : fo)) * 128, voffKP = ln < 2 ? 790528 + vpo : OOB, voffLP = ln < 2 ? 798720 + vpo : OOB;
  const u32 sKF = 790528 + (j * 16 + r) * 128, sLF = 798720 + (j * 16 + r) * 128, sSF = 1048576 + ((r * 4 + j) * 8 + w) * 128;
  A1 = MF32(xf[24], w204, A1);
  SB;
  const s16x8 w207 = TR2(ldsT1, 6144, 6656);
  asm volatile("" :: "v"(w204));
  float e4_3am = A0[6] * LOG2E;
  float e4_3bm = A0[7] * LOG2E;
  e4_3am = __builtin_fminf(e4_3am, 0.0f);
  e4_3bm = __builtin_fminf(e4_3bm, 0.0f);
  float e4_3ap = A0[6] + 1e-6f;
  float e4_3bp = A0[7] + 1e-6f;
  e4_3am = __builtin_amdgcn_exp2f(e4_3am);
  SB;
  A1 = MF32(xf[25], w205, A1);
  SB;
  const s16x8 w208 = TR2(ldsT1, 8192, 8704);
  asm volatile("" :: "v"(w205));
  e4_3bm = __builtin_amdgcn_exp2f(e4_3bm);
  e4_3ap = __builtin_fmaxf(e4_3ap, 1e-6f);
  e4_3bp = __builtin_fmaxf(e4_3bp, 1e-6f);
  e4_3ap = e4_3ap + e4_3am;
  e4_3bp = e4_3bp + e4_3bm;
  hp0[3] = cvtpk(e4_3ap, e4_3bp);
  SB;
  A1 = MF32(xf[26], w206, A1);
  SB;
  const s16x8 w209 = TR2(ldsT1, 10240, 10752);
  asm volatile("" :: "v"(w206));
  { u32x2 hq; hq[0] = hp0[2]; hq[1] = hp0[3]; *(LAS u32x2*)(size_t)(tq4 + (qa ^ 16)) = hq; }
  float e4_4am = A0[8] * LOG2E;
  float e4_4bm = A0[9] * LOG2E;
  e4_4am = __builtin_fminf(e4_4am, 0.0f);
  e4_4bm = __builtin_fminf(e4_4bm, 0.0f);
  float e4_4ap = A0[8] + 1e-6f;
  DMA2(lW, 24576, voffW6, srdW, sW, 1327104);
  SB;
  A1 = MF32(xf[27], w207, A1);
  SB;
  const s16x8 w210 = TR2(ldsT1, 12288, 12800);
  asm volatile("" :: "v"(w207));
  float e4_4bp = A0[9] + 1e-6f;
  e4_4am = __builtin_amdgcn_exp2f(e4_4am);
  e4_4bm = __builtin_amdgcn_exp2f(e4_4bm);
  e4_4ap = __builtin_fmaxf(e4_4ap, 1e-6f);
  e4_4bp = __builtin_fmaxf(e4_4bp, 1e-6f);
  SB;
  A1 = MF32(xf[28], w208, A1);
  SB;
  const s16x8 w211 = TR2(ldsT1, 14336, 14848);
  asm volatile("" :: "v"(w208));
  e4_4ap = e4_4ap + e4_4am;
  e4_4bp = e4_4bp + e4_4bm;
  hp1[0] = cvtpk(e4_4ap, e4_4bp);
  float e4_5am = A0[10] * LOG2E;
  float e4_5bm = A0[11] * LOG2E;
  e4_5am = __builtin_fminf(e4_5am, 0.0f);
  e4_5bm = __builtin_fminf(e4_5bm, 0.0f);
  float e4_5ap = A0[10] + 1e-6f;
  SB;
  A1 = MF32(xf[29], w209, A1);
  SB;
  const s16x8 w212 = TR2(ldsT1, 16384, 16896);
  asm volatile("" :: "v"(w209));
  float e4_5bp = A0[11] + 1e-6f;
  e4_5am = __builtin_amdgcn_exp2f(e4_5am);
  e4_5bm = __builtin_amdgcn_exp2f(e4_5bm);
  e4_5ap = __builtin_fmaxf(e4_5ap, 1e-6f);
  e4_5bp = __builtin_fmaxf(e4_5bp, 1e-6f);
  SB;
  SB; asm volatile("s_waitcnt vmcnt(1)\n\ts_barrier" :  :: "memory"); SB;
  FLAG_C(w, eq, 0, sKF, 1);
  A1 = MF32(xf[30], w210, A1);
  SB;
  const s16x8 w213 = TR2(ldsT1, 18432, 18944);
  asm volatile("" :: "v"(w210));
  e4_5ap = e4_5ap + e4_5am;
  e4_5bp = e4_5bp + e4_5bm;
  hp1[1] = cvtpk(e4_5ap, e4_5bp);
  { u32x2 hq; hq[0] = hp1[0]; hq[1] = hp1[1]; *(LAS u32x2*)(size_t)(tq4 + (qa ^ 32)) = hq; }
  float e4_6am = A0[12] * LOG2E;
  float e4_6bm = A0[13] * LOG2E;
  e4_6am = __builtin_fminf(e4_6am, 0.0f);
  DMA2(lW, 25600, voffW6, srdW, sW, 1354752);
  SB;
  A1 = MF32(xf[31], w211, A1);
  SB;
  const s16x8 w214 = TR2(ldsT1, 20480, 20992);
  asm volatile("" :: "v"(w211));
  e4_6bm = __builtin_fminf(e4_6bm, 0.0f);
  float e4_6ap = A0[12] + 1e-6f;
  float e4_6bp = A0[13] + 1e-6f;
  e4_6am = __builtin_amdgcn_exp2f(e4_6am);
  e4_6bm = __builtin_amdgcn_exp2f(e4_6bm);
  SB;
  A1 = MF32(xf[32], w212, A1);
  SB;
  const s16x8 w215 = TR2(ldsT1, 22528, 23040);
  asm volatile("" :: "v"(w212));
  e4_6ap = __builtin_fmaxf(e4_6ap, 1e-6f);
  e4_6bp = __builtin_fmaxf(e4_6bp, 1e-6f);
  e4_6ap = e4_6ap + e4_6am;
  e4_6bp = e4_6bp + e4_6bm;
  hp1[2] = cvtpk(e4_6ap, e4_6bp);
  float e4_7am = A0[14] * LOG2E;
  float e4_7bm = A0[15] * LOG2E;
  SB;
  A1 = MF32(xf[33], w213, A1);
  SB;
  const s16x8 w216 = TR2(ldsT1, 24576, 25088);
  asm volatile("" :: "v"(w213));
  e4_7am = __builtin_fminf(e4_7am, 0.0f);
  e4_7bm = __builtin_fminf(e4_7bm, 0.0f);
  float e4_7ap = A0[14] + 1e-6f;
  float e4_7bp = A0[15] + 1e-6f;
  e4_7am = __builtin_amdgcn_exp2f(e4_7am);
  e4_7bm = __builtin_amdgcn_exp2f(e4_7bm);
  SB;
  A1 = MF32(xf[34], w214, A1);
  SB;
  const s16x8 w217 = TR2(ldsT1, 26624, 27136);
  asm volatile("" :: "v"(w214));
  e4_7ap = __builtin_fmaxf(e4_7ap, 1e-6f);
  e4_7bp = __builtin_fmaxf(e4_7bp, 1e-6f);
  e4_7ap = e4_7ap + e4_7am;
  e4_7bp = e4_7bp + e4_7bm;
  hp1[3] = cvtpk(e4_7ap, e4_7bp);
  { u32x2 hq; hq[0] = hp1[2]; hq[1] = hp1[3]; *(LAS u32x2*)(size_t)(tq4 + (qa ^ 48)) = hq; }
  DMA2(lW, 26624, voffW6, srdW, sW, 1382400);
  SB;
  A1 = MF32(xf[35], w215, A1);
  SB;
  const s16x8 w218 = TR2(ldsT1, 28672, 29184);
  asm volatile("" :: "v"(w215));
  SB;
  SB; asm volatile("s_barrier" :  :: "memory"); SB;
  u32 voffL1, voffL1S, lzr, lzw; u32x2 l1m, plk, pll; u32x4 l1v[4]; s16x8 l1a; l1_setup(r, j, w, ln, voffL1, voffL1S, lzr, lzw, l1m, l1a);
  A0 = MF32(xf[0], w216, zero16);
  SB;
  const s16x8 w219 = TR2(ldsT1, 30720, 31232);
  asm volatile("" :: "v"(w216));
  SB;
  A0 = MF32(xf[1], w217, A0);
  SB;
  const s16x8 w220 = TR2(ldsT1, 32768, 33280);
  asm volatile("" :: "v"(w217));
  const u32 tq5 = (cg ? 139264 : 131072) + rg * 2048;
  float e5_0am = A1[0] * LOG2E;
  float e5_0bm = A1[1] * LOG2E;
  e5_0am = __builtin_fminf(e5_0am, 0.0f);
  SB;
  A0 = MF32(xf[2], w218, A0);
  SB;
  const s16x8 w221 = TR2(ldsT1, 34816, 35328);
  asm volatile("" :: "v"(w218));
  e5_0bm = __builtin_fminf(e5_0bm, 0.0f);
  float e5_0ap = A1[0] + 1e-6f;
  float e5_0bp = A1[1] + 1e-6f;
  e5_0am = __builtin_amdgcn_exp2f(e5_0am);
  LOAD2(plk, voffKP, srdWS, 0, 0, " sc1");
  SB;
  A0 = MF32(xf[3], w219, A0);
  SB;
  const s16x8 w222 = TR2(ldsT1, 36864, 37376);
  asm volatile("" :: "v"(w219));
  e5_0bm = __builtin_amdgcn_exp2f(e5_0bm);
  e5_0ap = __builtin_fmaxf(e5_0ap, 1e-6f);
  SB;
  A0 = MF32(xf[4], w220, A0);
  SB;
  const s16x8 w223 = TR2(ldsT1, 38912, 39424);
  asm volatile("" :: "v"(w220));
  e5_0bp = __builtin_fmaxf(e5_0bp, 1e-6f);
  e5_0ap = e5_0ap + e5_0am;
  e5_0bp = e5_0bp + e5_0bm;
  hp0[0] = cvtpk(e5_0ap, e5_0bp);
  SB;
  A0 = MF32(xf[5], w221, A0);
  SB;
  const s16x8 w224 = TR2(ldsT1, 40960, 41472);
  asm volatile("" :: "v"(w221));
  float e5_1am = A1[2] * LOG2E;
  float e5_1bm = A1[3] * LOG2E;
  e5_1am = __builtin_fminf(e5_1am, 0.0f);
  SB;
  A0 = MF32(xf[6], w222, A0);
  SB;
  const s16x8 w225 = TR2(ldsT1, 43008, 43520);
  asm volatile("" :: "v"(w222));
  e5_1bm = __builtin_fminf(e5_1bm, 0.0f);
  float e5_1ap = A1[2] + 1e-6f;
  float e5_1bp = A1[3] + 1e-6f;
  e5_1am = __builtin_amdgcn_exp2f(e5_1am);
  SB;
  A0 = MF32(xf[7], w223, A0);
  SB;
  const s16x8 w226 = TR2(ldsT1, 45056, 45568);
  asm volatile("" :: "v"(w223));
  e5_1bm = __builtin_amdgcn_exp2f(e5_1bm);
  e5_1ap = __builtin_fmaxf(e5_1ap, 1e-6f);
  SB;
  A0 = MF32(xf[8], w224, A0);
  SB;
  const s16x8 w227 = TR2(ldsT1, 47104, 47616);
  asm volatile("" :: "v"(w224));
  e5_1bp = __builtin_fmaxf(e5_1bp, 1e-6f);
  e5_1ap = e5_1ap + e5_1am;
  e5_1bp = e5_1bp + e5_1bm;
  hp0[1] = cvtpk(e5_1ap, e5_1bp);
  SB;
  SB; asm volatile("s_waitcnt vmcnt(0)" : "+v"(plk) :: "memory"); SB;
  POLL2(plk, voffKP, 1, 8);
  SB; asm volatile("s_barrier" :  :: "memory"); SB;
  LOAD4(l1v[0], voffL1, srdWS, 2097152, 0, " sc1");
  LOAD4(l1v[1], voffL1, srdWS, 2116096, 0, " sc1");
  LOAD4(l1v[2], voffL1, srdWS, 2135040, 0, " sc1");
  LOAD4(l1v[3], voffL1, srdWS, 2153984, 0, " sc1");
  A0 = MF32(xf[9], w225, A0);
  SB;
  const s16x8 w228 = TR2(ldsT2, 0, 512);
  asm volatile("" :: "v"(w225));
  { u32x2 hq; hq[0] = hp0[0]; hq[1] = hp0[1]; *(LAS u32x2*)(size_t)(tq5 + (qa ^ 0)) = hq; }
  float e5_2am = A1[4] * LOG2E;
  float e5_2bm = A1[5] * LOG2E;
  SB;
  A0 = MF32(xf[10], w226, A0);
  SB;
  const s16x8 w229 = TR2(ldsT2, 2048, 2560);
  asm volatile("" :: "v"(w226));
  e5_2am = __builtin_fminf(e5_2am, 0.0f);
  e5_2bm = __builtin_fminf(e5_2bm, 0.0f);
  float e5_2ap = A1[4] + 1e-6f;
  SB;
  A0 = MF32(xf[11], w227, A0);
  SB;
  const s16x8 w230 = TR2(ldsT2, 4096, 4608);
  asm volatile("" :: "v"(w227));
  float e5_2bp = A1[5] + 1e-6f;
  e5_2am = __builtin_amdgcn_exp2f(e5_2am);
  e5_2bm = __builtin_amdgcn_exp2f(e5_2bm);
  SB;
  SB; asm volatile("s_barrier" :  :: "memory"); SB;
  A0 = MF32(xf[12], w228, A0);
  SB;
  const s16x8 w231 = TR2(ldsT2, 6144, 6656);
  asm volatile("" :: "v"(w228));
  e5_2ap = __builtin_fmaxf(e5_2ap, 1e-6f);
  e5_2bp = __builtin_fmaxf(e5_2bp, 1e-6f);
  e5_2ap = e5_2ap + e5_2am;
  SB;
  A0 = MF32(xf[13], w229, A0);
  SB;
  const s16x8 w232 = TR2(ldsT2, 8192, 8704);
  asm volatile("" :: "v"(w229));
  e5_2bp = e5_2bp + e5_2bm;
  hp0[2] = cvtpk(e5_2ap, e5_2bp);
  float e5_3am = A1[6] * LOG2E;
  float e5_3bm = A1[7] * LOG2E;
  SB;
  A0 = MF32(xf[14], w230, A0);
  SB;
  const s16x8 w233 = TR2(ldsT2, 10240, 10752);
  asm volatile("" :: "v"(w230));
  e5_3am = __builtin_fminf(e5_3am, 0.0f);
  e5_3bm = __builtin_fminf(e5_3bm, 0.0f);
  float e5_3ap = A1[6] + 1e-6f;
  SB;
  A0 = MF32(xf[15], w231, A0);
  SB;
  const s16x8 w234 = TR2(ldsT2, 12288, 12800);
  asm volatile("" :: "v"(w231));
  float e5_3bp = A1[7] + 1e-6f;
  e5_3am = __builtin_amdgcn_exp2f(e5_3am);
  e5_3bm = __builtin_amdgcn_exp2f(e5_3bm);
  SB;
  A0 = MF32(xf[16], w232, A0);
  SB;
  const s16x8 w235 = TR2(ldsT2, 14336, 14848);
  asm volatile("" :: "v"(w232));
  e5_3ap = __builtin_fmaxf(e5_3ap, 1e-6f);
  e5_3bp = __builtin_fmaxf(e5_3bp, 1e-6f);
  e5_3ap = e5_3ap + e5_3am;
  SB;
  SB; asm volatile("s_waitcnt vmcnt(0)" : "+v"(l1v[0]), "+v"(l1v[1]), "+v"(l1v[2]), "+v"(l1v[3]) :: "memory"); SB;
  f32x4 l1s = MF16(l1a, FR(l1v[0]), (f32x4{0.f, 0.f, 0.f, 0.f})); l1s = MF16(l1a, FR(l1v[1]), l1s); l1s = MF16(l1a, FR(l1v[2]), l1s); l1s = MF16(l1a, FR(l1v[3]), l1s); SB;
  A0 = MF32(xf[17], w233, A0);
  SB;
  const s16x8 w236 = TR2(ldsT2, 16384, 16896);
  asm volatile("" :: "v"(w233));
  e5_3bp = e5_3bp + e5_3bm;
  hp0[3] = cvtpk(e5_3ap, e5_3bp);
  { u32x2 hq; hq[0] = hp0[2]; hq[1] = hp0[3]; *(LAS u32x2*)(size_t)(tq5 + (qa ^ 16)) = hq; }
  SB;
  A0 = MF32(xf[18], w234, A0);
  SB;
  const s16x8 w237 = TR2(ldsT2, 18432, 18944);
  asm volatile("" :: "v"(w234));
  float e5_4am = A1[8] * LOG2E;
  float e5_4bm = A1[9] * LOG2E;
  e5_4am = __builtin_fminf(e5_4am, 0.0f);
  e5_4bm = __builtin_fminf(e5_4bm, 0.0f);
  SB;
  A0 = MF32(xf[19], w235, A0);
  SB;
  const s16x8 w238 = TR2(ldsT2, 20480, 20992);
  asm volatile("" :: "v"(w235));
  float e5_4ap = A1[8] + 1e-6f;
  float e5_4bp = A1[9] + 1e-6f;
  e5_4am = __builtin_amdgcn_exp2f(e5_4am);
  u32x2 l1k; l1k[0] = cvtb(l1s[0], l1s[1]); l1k[1] = cvtb(l1s[2], l1s[3]); *(LAS u32x2*)(size_t)(lzw) = l1k; l1k &= l1m;
  SB;
  A0 = MF32(xf[20], w236, A0);
  SB;
  const s16x8 w239 = TR2(ldsT2, 22528, 23040);
  asm volatile("" :: "v"(w236));
  e5_4bm = __builtin_amdgcn_exp2f(e5_4bm);
  e5_4ap = __builtin_fmaxf(e5_4ap, 1e-6f);
  STORE2(l1k, voffL1S, srdWS, 1572864, 0);
  SB;
  A0 = MF32(xf[21], w237, A0);
  SB;
  const s16x8 w240 = TR2(ldsT0, 24576, 25088);
  asm volatile("" :: "v"(w237));
  e5_4bp = __builtin_fmaxf(e5_4bp, 1e-6f);
  e5_4ap = e5_4ap + e5_4am;
  e5_4bp = e5_4bp + e5_4bm;
  hp1[0] = cvtpk(e5_4ap, e5_4bp);
  SB;
  A0 = MF32(xf[22], w238, A0);
  SB;
  const s16x8 w241 = TR2(ldsT0, 26624, 27136);
  asm volatile("" :: "v"(w238));
  float e5_5am = A1[10] * LOG2E;
  float e5_5bm = A1[11] * LOG2E;
  e5_5am = __builtin_fminf(e5_5am, 0.0f);
  e5_5bm = __builtin_fminf(e5_5bm, 0.0f);
  SB;
  A0 = MF32(xf[23], w239, A0);
  SB;
  const s16x8 w242 = TR2(ldsT0, 28672, 29184);
  asm volatile("" :: "v"(w239));
  float e5_5ap = A1[10] + 1e-6f;
  float e5_5bp = A1[11] + 1e-6f;
  e5_5am = __builtin_amdgcn_exp2f(e5_5am);
  SB;
  SB; asm volatile("s_waitcnt lgkmcnt(0)\n\ts_barrier" :  :: "memory"); SB;
  A0 = MF32(xf[24], w240, A0);
  SB;
  const s16x8 w243 = TR2(ldsT0, 30720, 31232);
  asm volatile("" :: "v"(w240));
  e5_5bm = __builtin_amdgcn_exp2f(e5_5bm);
  e5_5ap = __builtin_fmaxf(e5_5ap, 1e-6f);
  u32x2 l1z = *(LAS u32x2*)(size_t)(lzr);
  SB;
  A0 = MF32(xf[25], w241, A0);
  SB;
  const s16x8 w244 = TR2(ldsT0, 32768, 33280);
  asm volatile("" :: "v"(w241));
  e5_5bp = __builtin_fmaxf(e5_5bp, 1e-6f);
  e5_5ap = e5_5ap + e5_5am;
  e5_5bp = e5_5bp + e5_5bm;
  hp1[1] = cvtpk(e5_5ap, e5_5bp);
  l1z &= l1m;
  STORE2(l1z, voffL1S, srdWS, 1591296, 0);
  SB;
  A0 = MF32(xf[26], w242, A0);
  SB;
  const s16x8 w245 = TR2(ldsT0, 34816, 35328);
  asm volatile("" :: "v"(w242));
  { u32x2 hq; hq[0] = hp1[0]; hq[1] = hp1[1]; *(LAS u32x2*)(size_t)(tq5 + (qa ^ 32)) = hq; }
  float e5_6am = A1[12] * LOG2E;
  float e5_6bm = A1[13] * LOG2E;
  SB;
  A0 = MF32(xf[27], w243, A0);
  SB;
  const s16x8 w246 = TR2(ldsT0, 36864, 37376);
  asm volatile("" :: "v"(w243));
  e5_6am = __builtin_fminf(e5_6am, 0.0f);
  e5_6bm = __builtin_fminf(e5_6bm, 0.0f);
  float e5_6ap = A1[12] + 1e-6f;
  float e5_6bp = A1[13] + 1e-6f;
  SB;
  A0 = MF32(xf[28], w244, A0);
  SB;
  const s16x8 w247 = TR2(ldsT0, 38912, 39424);
  asm volatile("" :: "v"(w244));
  e5_6am = __builtin_amdgcn_exp2f(e5_6am);
  e5_6bm = __builtin_amdgcn_exp2f(e5_6bm);
  SB;
  A0 = MF32(xf[29], w245, A0);
  SB;
  const s16x8 w248 = TR2(ldsT0, 40960, 41472);
  asm volatile("" :: "v"(w245));
  e5_6ap = __builtin_fmaxf(e5_6ap, 1e-6f);
  e5_6bp = __builtin_fmaxf(e5_6bp, 1e-6f);
  e5_6ap = e5_6ap + e5_6am;
  SB;
  A0 = MF32(xf[30], w246, A0);
  SB;
  const s16x8 w249 = TR2(ldsT0, 43008, 43520);
  asm volatile("" :: "v"(w246));
  e5_6bp = e5_6bp + e5_6bm;
  hp1[2] = cvtpk(e5_6ap, e5_6bp);
  float e5_7am = A1[14] * LOG2E;
  float e5_7bm = A1[15] * LOG2E;
  SB;
  A0 = MF32(xf[31], w247, A0);
  SB;
  const s16x8 w250 = TR2(ldsT0, 45056, 45568);
  asm volatile("" :: "v"(w247));
  e5_7am = __builtin_fminf(e5_7am, 0.0f);
  e5_7bm = __builtin_fminf(e5_7bm, 0.0f);
  float e5_7ap = A1[14] + 1e-6f;
  float e5_7bp = A1[15] + 1e-6f;
  SB;
  SB; asm volatile("s_waitcnt vmcnt(0)\n\ts_barrier" :  :: "memory"); SB;
  FLAG_C(w, eq, 0, sLF, 1);
  A0 = MF32(xf[32], w248, A0);
  SB;
  const s16x8 w251 = TR2(ldsT0, 47104, 47616);
  asm volatile("" :: "v"(w248));
  e5_7am = __builtin_amdgcn_exp2f(e5_7am);
  e5_7bm = __builtin_amdgcn_exp2f(e5_7bm);
  SB;
  A0 = MF32(xf[33], w249, A0);
  SB;
  asm volatile("" :: "v"(w249));
  e5_7ap = __builtin_fmaxf(e5_7ap, 1e-6f);
  e5_7bp = __builtin_fmaxf(e5_7bp, 1e-6f);
  e5_7ap = e5_7ap + e5_7am;
  SB;
  A0 = MF32(xf[34], w250, A0);
  SB;
  asm volatile("" :: "v"(w250));
  e5_7bp = e5_7bp + e5_7bm;
  hp1[3] = cvtpk(e5_7ap, e5_7bp);
  { u32x2 hq; hq[0] = hp1[2]; hq[1] = hp1[3]; *(LAS u32x2*)(size_t)(tq5 + (qa ^ 48)) = hq; }
  SB;
  A0 = MF32(xf[35], w251, A0);
  SB;
  asm volatile("" :: "v"(w251));
  SB;
  const u32 tq6 = (cg ? 155648 : 147456) + rg * 2048;
  float e6_0am = A0[0] * LOG2E;
  float e6_0bm = A0[1] * LOG2E;
  e6_0am = __builtin_fminf(e6_0am, 0.0f);
  e6_0bm = __builtin_fminf(e6_0bm, 0.0f);
  float e6_0ap = A0[0] + 1e-6f;
  float e6_0bp = A0[1] + 1e-6f;
  e6_0am = __builtin_amdgcn_exp2f(e6_0am);
  e6_0bm = __builtin_amdgcn_exp2f(e6_0bm);
  e6_0ap = __builtin_fmaxf(e6_0ap, 1e-6f);
  e6_0bp = __builtin_fmaxf(e6_0bp, 1e-6f);
  e6_0ap = e6_0ap + e6_0am;
  e6_0bp = e6_0bp + e6_0bm;
  hp0[0] = cvtpk(e6_0ap, e6_0bp);
  float e6_1am = A0[2] * LOG2E;
  float e6_1bm = A0[3] * LOG2E;
  e6_1am = __builtin_fminf(e6_1am, 0.0f);
  e6_1bm = __builtin_fminf(e6_1bm, 0.0f);
  float e6_1ap = A0[2] + 1e-6f;
  float e6_1bp = A0[3] + 1e-6f;
  e6_1am = __builtin_amdgcn_exp2f(e6_1am);
  e6_1bm = __builtin_amdgcn_exp2f(e6_1bm);
  e6_1ap = __builtin_fmaxf(e6_1ap, 1e-6f);
  e6_1bp = __builtin_fmaxf(e6_1bp, 1e-6f);
  e6_1ap = e6_1ap + e6_1am;
  e6_1bp = e6_1bp + e6_1bm;
  hp0[1] = cvtpk(e6_1ap, e6_1bp);
  { u32x2 hq; hq[0] = hp0[0]; hq[1] = hp0[1]; *(LAS u32x2*)(size_t)(tq6 + (qa ^ 0)) = hq; }
  float e6_2am = A0[4] * LOG2E;
  float e6_2bm = A0[5] * LOG2E;
  e6_2am = __builtin_fminf(e6_2am, 0.0f);
  e6_2bm = __builtin_fminf(e6_2bm, 0.0f);
  float e6_2ap = A0[4] + 1e-6f;
  float e6_2bp = A0[5] + 1e-6f;
  e6_2am = __builtin_amdgcn_exp2f(e6_2am);
  e6_2bm = __builtin_amdgcn_exp2f(e6_2bm);
  e6_2ap = __builtin_fmaxf(e6_2ap, 1e-6f);
  e6_2bp = __builtin_fmaxf(e6_2bp, 1e-6f);
  e6_2ap = e6_2ap + e6_2am;
  e6_2bp = e6_2bp + e6_2bm;
  hp0[2] = cvtpk(e6_2ap, e6_2bp);
  float e6_3am = A0[6] * LOG2E;
  float e6_3bm = A0[7] * LOG2E;
  SB; spin2(srdWS, voffLP, tagLo, tagHi, 1);
  SB; asm volatile("s_waitcnt vmcnt(0)\n\ts_barrier" :  :: "memory"); SB;
  const u32 sL2 = 1572864 + j * 36864 + w * 1024, lq = 122880 + cg * 16384 + rg * 2048 + (4 * hh + q4) * 64;
  const u32 lq0 = OPQ(lq + (((4 * ((ln >> 4) & 1) + (ln & 3)) ^ (2 * hh + (q4 >> 1))) << 3)), lq1 = OPQ(lq + 512 + (((4 * ((ln >> 4) & 1) + (ln & 3)) ^ (4 + 2 * hh + (q4 >> 1))) << 3));
  u32x4 l2v[5]; const u32 lkw = OPQ(0 + w * 1024 + vl16), lkf = OPQ(0 + cg * 8192 + vl16);
  const u32 sOo = 16777216 + ((r * 4 + j) * 5 + 2 * cg) * 8192, voffH0 = cg ? OOB : voffH;
  LOAD4(l2v[0], vl16, srdWS, sL2 + 0, 0, " sc1");
  LOAD4(l2v[1], vl16, srdWS, sL2 + 8192, 0, " sc1");
  LOAD4(l2v[2], vl16, srdWS, sL2 + 16384, 0, " sc1");
  LOAD4(l2v[3], vl16, srdWS, sL2 + 24576, 0, " sc1");
  LOAD4_C(cg, eq, 0, l2v[4], vl16, srdWS, sL2 + 32768, 0, " sc1");
  SB;
  e6_3am = __builtin_fminf(e6_3am, 0.0f);
  e6_3bm = __builtin_fminf(e6_3bm, 0.0f);
  float e6_3ap = A0[6] + 1e-6f;
  float e6_3bp = A0[7] + 1e-6f;
  e6_3am = __builtin_amdgcn_exp2f(e6_3am);
  e6_3bm = __builtin_amdgcn_exp2f(e6_3bm);
  e6_3ap = __builtin_fmaxf(e6_3ap, 1e-6f);
  e6_3bp = __builtin_fmaxf(e6_3bp, 1e-6f);
  e6_3ap = e6_3ap + e6_3am;
  e6_3bp = e6_3bp + e6_3bm;
  hp0[3] = cvtpk(e6_3ap, e6_3bp);
  { u32x2 hq; hq[0] = hp0[2]; hq[1] = hp0[3]; *(LAS u32x2*)(size_t)(tq6 + (qa ^ 16)) = hq; }
  float e6_4am = A0[8] * LOG2E;
  float e6_4bm = A0[9] * LOG2E;
  e6_4am = __builtin_fminf(e6_4am, 0.0f);
  e6_4bm = __builtin_fminf(e6_4bm, 0.0f);
  float e6_4ap = A0[8] + 1e-6f;
  float e6_4bp = A0[9] + 1e-6f;
  e6_4am = __builtin_amdgcn_exp2f(e6_4am);
  e6_4bm = __builtin_amdgcn_exp2f(e6_4bm);
  e6_4ap = __builtin_fmaxf(e6_4ap, 1e-6f);
  e6_4bp = __builtin_fmaxf(e6_4bp, 1e-6f);
  e6_4ap = e6_4ap + e6_4am;
  e6_4bp = e6_4bp + e6_4bm;
  hp1[0] = cvtpk(e6_4ap, e6_4bp);
  float e6_5am = A0[10] * LOG2E;
  float e6_5bm = A0[11] * LOG2E;
  e6_5am = __builtin_fminf(e6_5am, 0.0f);
  e6_5bm = __builtin_fminf(e6_5bm, 0.0f);
  float e6_5ap = A0[10] + 1e-6f;
  float e6_5bp = A0[11] + 1e-6f;
  e6_5am = __builtin_amdgcn_exp2f(e6_5am);
  e6_5bm = __builtin_amdgcn_exp2f(e6_5bm);
  e6_5ap = __builtin_fmaxf(e6_5ap, 1e-6f);
  e6_5bp = __builtin_fmaxf(e6_5bp, 1e-6f);
  e6_5ap = e6_5ap + e6_5am;
  e6_5bp = e6_5bp + e6_5bm;
  hp1[1] = cvtpk(e6_5ap, e6_5bp);
  { u32x2 hq; hq[0] = hp1[0]; hq[1] = hp1[1]; *(LAS u32x2*)(size_t)(tq6 + (qa ^ 32)) = hq; }
  float e6_6am = A0[12] * LOG2E;
  float e6_6bm = A0[13] * LOG2E;
  e6_6am = __builtin_fminf(e6_6am, 0.0f);
  e6_6bm = __builtin_fminf(e6_6bm, 0.0f);
  float e6_6ap = A0[12] + 1e-6f;
  float e6_6bp = A0[13] + 1e-6f;
  e6_6am = __builtin_amdgcn_exp2f(e6_6am);
  e6_6bm = __builtin_amdgcn_exp2f(e6_6bm);
  e6_6ap = __builtin_fmaxf(e6_6ap, 1e-6f);
  e6_6bp = __builtin_fmaxf(e6_6bp, 1e-6f);
  e6_6ap = e6_6ap + e6_6am;
  e6_6bp = e6_6bp + e6_6bm;
  hp1[2] = cvtpk(e6_6ap, e6_6bp);
  float e6_7am = A0[14] * LOG2E;
  float e6_7bm = A0[15] * LOG2E;
  e6_7am = __builtin_fminf(e6_7am, 0.0f);
  e6_7bm = __builtin_fminf(e6_7bm, 0.0f);
  float e6_7ap = A0[14] + 1e-6f;
  float e6_7bp = A0[15] + 1e-6f;
  e6_7am = __builtin_amdgcn_exp2f(e6_7am);
  e6_7bm = __builtin_amdgcn_exp2f(e6_7bm);
  e6_7ap = __builtin_fmaxf(e6_7ap, 1e-6f);
  e6_7bp = __builtin_fmaxf(e6_7bp, 1e-6f);
  e6_7ap = e6_7ap + e6_7am;
  e6_7bp = e6_7bp + e6_7bm;
  hp1[3] = cvtpk(e6_7ap, e6_7bp);
  { u32x2 hq; hq[0] = hp1[2]; hq[1] = hp1[3]; *(LAS u32x2*)(size_t)(tq6 + (qa ^ 48)) = hq; }
  SB; asm volatile("s_waitcnt vmcnt(0)" : "+v"(l2v[0]), "+v"(l2v[1]), "+v"(l2v[2]), "+v"(l2v[3]), "+v"(l2v[4]) :: "memory"); SB;
  *(LAS u32x4*)(size_t)(lkw + 0) = l2v[0];
  *(LAS u32x4*)(size_t)(lkw + 8192) = l2v[1];
  *(LAS u32x4*)(size_t)(lkw + 16384) = l2v[2];
  *(LAS u32x4*)(size_t)(lkw + 24576) = l2v[3];
  if (cg == 0) *(LAS u32x4*)(size_t)(lkw + 32768) = l2v[4];
  SB; asm volatile("s_waitcnt lgkmcnt(0)\n\ts_barrier" :  :: "memory"); SB;
  const s16x8 pq0_0 = TR2B(lq0, 0, lq1, 0), pq0_1 = TR2B(lq0, 1024, lq1, 1024);
  const s16x8 pq1_0 = TR2B(lq0, 8192, lq1, 8192), pq1_1 = TR2B(lq0, 9216, lq1, 9216);
  const s16x8 pq2_0 = TR2B(lq0, 16384, lq1, 16384), pq2_1 = TR2B(lq0, 17408, lq1, 17408);
  f32x16 eps16; for (int i = 0; i < 16; ++i) eps16[i] = 1e-6f;
  const s16x8 of0 = *(LAS s16x8*)(size_t)(lkf + 20480);
  const s16x8 of1 = *(LAS s16x8*)(size_t)(lkf + 2048);
  const s16x8 of2 = *(LAS s16x8*)(size_t)(lkf + 21504);
  const s16x8 of3 = *(LAS s16x8*)(size_t)(lkf + 3072);
  f32x16 den1 = MF32(of0, pq0_0, eps16); SB;
  const s16x8 of4 = *(LAS s16x8*)(size_t)(lkf + 22528);
  SB;
  f32x16 num1 = MF32(of1, pq0_0, zero16); SB;
  const s16x8 of5 = *(LAS s16x8*)(size_t)(lkf + 4096);
  SB;
  den1 = MF32(of2, pq0_1, den1); SB;
  const s16x8 of6 = *(LAS s16x8*)(size_t)(lkf + 23552);
  SB;
  num1 = MF32(of3, pq0_1, num1); SB;
  const s16x8 of7 = *(LAS s16x8*)(size_t)(lkf + 5120);
  SB;
  den1 = MF32(of4, pq1_0, den1); SB;
  const s16x8 of8 = *(LAS s16x8*)(size_t)(lkf + 24576);
  SB;
  num1 = MF32(of5, pq1_0, num1); SB;
  const s16x8 of9 = *(LAS s16x8*)(size_t)(lkf + 6144);
  SB;
  den1 = MF32(of6, pq1_1, den1); SB;
  const s16x8 of10 = *(LAS s16x8*)(size_t)(lkf + 25600);
  SB;
  num1 = MF32(of7, pq1_1, num1); SB;
  const s16x8 of11 = *(LAS s16x8*)(size_t)(lkf + 7168);
  SB;
  f32x16 den2 = MF32(of8, pq1_0, eps16); SB;
  const s16x8 of12 = *(LAS s16x8*)(size_t)(lkf + 26624);
  SB;
  f32x16 num2 = MF32(of9, pq1_0, zero16); SB;
  const s16x8 of13 = *(LAS s16x8*)(size_t)(lkf + 8192);
  SB;
  den2 = MF32(of10, pq1_1, den2); SB;
  const s16x8 of14 = *(LAS s16x8*)(size_t)(lkf + 27648);
  float o1_0a = __builtin_amdgcn_rcpf(den1[0]), o1_0b = __builtin_amdgcn_rcpf(den1[1]);
  o1_0a *= num1[0]; o1_0b *= num1[1];
  hp0[0] = cvtpk(o1_0a, o1_0b);
  float o1_1a = __builtin_amdgcn_rcpf(den1[2]), o1_1b = __builtin_amdgcn_rcpf(den1[3]);
  SB;
  num2 = MF32(of11, pq1_1, num2); SB;
  const s16x8 of15 = *(LAS s16x8*)(size_t)(lkf + 9216);
  o1_1a *= num1[2]; o1_1b *= num1[3];
  hp0[1] = cvtpk(o1_1a, o1_1b);
  float o1_2a = __builtin_amdgcn_rcpf(den1[4]), o1_2b = __builtin_amdgcn_rcpf(den1[5]);
  SB;
  den2 = MF32(of12, pq2_0, den2); SB;
  const s16x8 of16 = *(LAS s16x8*)(size_t)(lkf + 18432);
  o1_2a *= num1[4]; o1_2b *= num1[5];
  hp0[2] = cvtpk(o1_2a, o1_2b);
  float o1_3a = __builtin_amdgcn_rcpf(den1[6]), o1_3b = __builtin_amdgcn_rcpf(den1[7]);
  o1_3a *= num1[6]; o1_3b *= num1[7];
  hp0[3] = cvtpk(o1_3a, o1_3b);
  STORE4(hp0, voffH, srdWS, sOo + 8192, 0);
  SB;
  num2 = MF32(of13, pq2_0, num2); SB;
  const s16x8 of17 = *(LAS s16x8*)(size_t)(lkf + 0);
  float o1_4a = __builtin_amdgcn_rcpf(den1[8]), o1_4b = __builtin_amdgcn_rcpf(den1[9]);
  o1_4a *= num1[8]; o1_4b *= num1[9];
  hp1[0] = cvtpk(o1_4a, o1_4b);
  float o1_5a = __builtin_amdgcn_rcpf(den1[10]), o1_5b = __builtin_amdgcn_rcpf(den1[11]);
  SB;
  den2 = MF32(of14, pq2_1, den2); SB;
  const s16x8 of18 = *(LAS s16x8*)(size_t)(lkf + 19456);
  o1_5a *= num1[10]; o1_5b *= num1[11];
  hp1[1] = cvtpk(o1_5a, o1_5b);
  float o1_6a = __builtin_amdgcn_rcpf(den1[12]), o1_6b = __builtin_amdgcn_rcpf(den1[13]);
  SB;
  num2 = MF32(of15, pq2_1, num2); SB;
  const s16x8 of19 = *(LAS s16x8*)(size_t)(lkf + 1024);
  o1_6a *= num1[12]; o1_6b *= num1[13];
  hp1[2] = cvtpk(o1_6a, o1_6b);
  float o1_7a = __builtin_amdgcn_rcpf(den1[14]), o1_7b = __builtin_amdgcn_rcpf(den1[15]);
  o1_7a *= num1[14]; o1_7b *= num1[15];
  hp1[3] = cvtpk(o1_7a, o1_7b);
  STORE4(hp1, voffH, srdWS, sOo + 8192, 512);
  SB;
  f32x16 den0 = MF32(of16, pq0_0, eps16); SB;
  SB;
  f32x16 num0 = MF32(of17, pq0_0, zero16); SB;
  SB;
  den0 = MF32(of18, pq0_1, den0); SB;
  float o2_0a = __builtin_amdgcn_rcpf(den2[0]), o2_0b = __builtin_amdgcn_rcpf(den2[1]);
  o2_0a *= num2[0]; o2_0b *= num2[1];
  hp0[0] = cvtpk(o2_0a, o2_0b);
  float o2_1a = __builtin_amdgcn_rcpf(den2[2]), o2_1b = __builtin_amdgcn_rcpf(den2[3]);
  o2_1a *= num2[2]; o2_1b *= num2[3];
  hp0[1] = cvtpk(o2_1a, o2_1b);
  float o2_2a = __builtin_amdgcn_rcpf(den2[4]), o2_2b = __builtin_amdgcn_rcpf(den2[5]);
  o2_2a *= num2[4]; o2_2b *= num2[5];
  hp0[2] = cvtpk(o2_2a, o2_2b);
  float o2_3a = __builtin_amdgcn_rcpf(den2[6]), o2_3b = __builtin_amdgcn_rcpf(den2[7]);
  o2_3a *= num2[6]; o2_3b *= num2[7];
  hp0[3] = cvtpk(o2_3a, o2_3b);
  STORE4(hp0, voffH, srdWS, sOo + 16384, 0);
  SB;
  num0 = MF32(of19, pq0_1, num0); SB;
  float o2_4a = __builtin_amdgcn_rcpf(den2[8]), o2_4b = __builtin_amdgcn_rcpf(den2[9]);
  o2_4a *= num2[8]; o2_4b *= num2[9];
  hp1[0] = cvtpk(o2_4a, o2_4b);
  float o2_5a = __builtin_amdgcn_rcpf(den2[10]), o2_5b = __builtin_amdgcn_rcpf(den2[11]);
  o2_5a *= num2[10]; o2_5b *= num2[11];
  hp1[1] = cvtpk(o2_5a, o2_5b);
  float o2_6a = __builtin_amdgcn_rcpf(den2[12]), o2_6b = __builtin_amdgcn_rcpf(den2[13]);
  o2_6a *= num2[12]; o2_6b *= num2[13];
  hp1[2] = cvtpk(o2_6a, o2_6b);
  float o2_7a = __builtin_amdgcn_rcpf(den2[14]), o2_7b = __builtin_amdgcn_rcpf(den2[15]);
  o2_7a *= num2[14]; o2_7b *= num2[15];
  hp1[3] = cvtpk(o2_7a, o2_7b);
  STORE4(hp1, voffH, srdWS, sOo + 16384, 512);
  SB;
  float o0_0a = __builtin_amdgcn_rcpf(den0[0]), o0_0b = __builtin_amdgcn_rcpf(den0[1]);
  o0_0a *= num0[0]; o0_0b *= num0[1];
  hp0[0] = cvtpk(o0_0a, o0_0b);
  float o0_1a = __builtin_amdgcn_rcpf(den0[2]), o0_1b = __builtin_amdgcn_rcpf(den0[3]);
  o0_1a *= num0[2]; o0_1b *= num0[3];
  hp0[1] = cvtpk(o0_1a, o0_1b);
  float o0_2a = __builtin_amdgcn_rcpf(den0[4]), o0_2b = __builtin_amdgcn_rcpf(den0[5]);
  o0_2a *= num0[4]; o0_2b *= num0[5];
  hp0[2] = cvtpk(o0_2a, o0_2b);
  float o0_3a = __builtin_amdgcn_rcpf(den0[6]), o0_3b = __builtin_amdgcn_rcpf(den0[7]);
  o0_3a *= num0[6]; o0_3b *= num0[7];
  hp0[3] = cvtpk(o0_3a, o0_3b);
  STORE4(hp0, voffH0, srdWS, sOo + 0, 0);
  float o0_4a = __builtin_amdgcn_rcpf(den0[8]), o0_4b = __builtin_amdgcn_rcpf(den0[9]);
  o0_4a *= num0[8]; o0_4b *= num0[9];
  hp1[0] = cvtpk(o0_4a, o0_4b);
  float o0_5a = __builtin_amdgcn_rcpf(den0[10]), o0_5b = __builtin_amdgcn_rcpf(den0[11]);
  o0_5a *= num0[10]; o0_5b *= num0[11];
  hp1[1] = cvtpk(o0_5a, o0_5b);
  float o0_6a = __builtin_amdgcn_rcpf(den0[12]), o0_6b = __builtin_amdgcn_rcpf(den0[13]);
  o0_6a *= num0[12]; o0_6b *= num0[13];
  hp1[2] = cvtpk(o0_6a, o0_6b);
  float o0_7a = __builtin_amdgcn_rcpf(den0[14]), o0_7b = __builtin_amdgcn_rcpf(den0[15]);
  o0_7a *= num0[14]; o0_7b *= num0[15];
  hp1[3] = cvtpk(o0_7a, o0_7b);
  STORE4(hp1, voffH0, srdWS, sOo + 0, 512);
  SB; asm volatile("s_waitcnt vmcnt(0)" :  :: "memory"); SB;
  FLAG(sSF, 1);
    if (bid == 0 && w == 0) epoch_update(srdWS, ln, tagLo, tagHi);
  }
  asm volatile("L_code_end:" ::: "memory");
}

extern "C" void kernel_launch(void* const* d_in, const int* in_sizes, int n_in, void* d_out, int out_size, void* d_ws, size_t ws_size,
                              hipStream_t stream) {
  (void)in_sizes; (void)n_in; (void)out_size; (void)ws_size;
  (void)hipFuncSetAttribute((const void*)fused_block, hipFuncAttributeMaxDynamicSharedMemorySize, 163840);
  fused_block<<<256, 512, 163840, stream>>>((const u16*)d_in[0], (const u16*)d_in[1], (const u16*)d_in[2], (const u16*)d_in[4],
                                            (const u16*)d_in[5], (const u16*)d_in[6], (const u16*)d_in[7], (u16*)d_out, (char*)d_ws);
}
```
